```python
import torch

def _softmax(x, axis=-1):
    return torch.softmax(x, dim=axis)
def _gelu(x):
    return torch.nn.functional.gelu(x, approximate="tanh")
def _silu(x):
    return torch.nn.functional.silu(x)
def _sigmoid(x):
    return torch.sigmoid(x)
def _layernorm(x, axis=-1, eps=1e-5):
    m = x.mean(dim=axis, keepdim=True)
    v = x.var(dim=axis, keepdim=True, unbiased=False)
    return (x - m) / torch.sqrt(v + eps)
def _scatter_add(tgt, idx, src, axis=0):
    return tgt.clone().index_add_(axis, idx, src)
def _segment_sum(src, seg, n):
    out = torch.zeros((n,) + src.shape[1:], dtype=src.dtype)
    return out.index_add_(0, seg, src)
def _pad(x, pw):
    flat = tuple(v for pair in reversed(pw) for v in pair)
    return torch.nn.functional.pad(x, flat)

kernel_name = 'standalone__low_rank_proj_linformer__sampling'

def setup_inputs():

    _w = (lambda a: (lambda t: t.to(torch.bfloat16) if t.is_floating_point() else t)(torch.as_tensor(a)))
    B, H, Sq, Skv, Dh, R = 8, 32, 4, 2048, 128, 64
    return {
        "q": _w((lambda *s: torch.randn(*s, dtype=torch.bfloat16) * 0.8)(B, H, Sq, Dh)),
        "k": _w((lambda *s: torch.randn(*s, dtype=torch.bfloat16) * 0.8)(B, H, Skv, Dh)),
        "v": _w((lambda *s: torch.randn(*s, dtype=torch.bfloat16) * 0.05)(B, H, Skv, Dh)),
        "e_proj": _w((lambda *s: torch.randn(*s, dtype=torch.bfloat16) * 0.8)(R, Skv) * (Skv ** -0.5)),
        "f_proj": _w((lambda *s: torch.randn(*s, dtype=torch.bfloat16) * 0.05)(R, Skv) * (Skv ** -0.5)),
    }

def reference(q, k, v, e_proj, f_proj):
    B, H, Sq, Dh = q.shape
    kp = torch.einsum("rn,bhnd->bhrd", e_proj, k)
    vp = torch.einsum("rn,bhnd->bhrd", f_proj, v)
    scores = torch.einsum("bhqd,bhrd->bhqr", q, kp) * (Dh ** -0.5)
    p = _softmax(scores, axis=-1)
    o = torch.einsum("bhqr,bhrd->bhqd", p, vp)
    return o
```

```cpp
#include <hip/hip_runtime.h>
#include <hip/hip_bf16.h>

typedef unsigned u32x4 __attribute__((ext_vector_type(4)));
typedef unsigned u32x2 __attribute__((ext_vector_type(2)));
typedef float f32x4 __attribute__((ext_vector_type(4)));
typedef __bf16 bf16x8 __attribute__((ext_vector_type(8)));
typedef __bf16 bf16x2 __attribute__((ext_vector_type(2)));
#define DI __device__ __forceinline__

#define NBUF 4
constexpr int SKV = 2048, DH = 128, RR = 64, SQ = 4, NW = 4;
constexpr int HEAD_BYTES = SKV * DH * 2;
constexpr int NB = SKV / 32 / NW;

DI f32x4 mfma(u32x4 a, u32x4 b, f32x4 c) {
  return __builtin_amdgcn_mfma_f32_16x16x32_bf16(__builtin_bit_cast(bf16x8, a), __builtin_bit_cast(bf16x8, b), c, 0, 0, 0);
}
DI unsigned pk(float lo, float hi) {
  bf16x2 v = {(__bf16)lo, (__bf16)hi};
  return __builtin_bit_cast(unsigned, v);
}

#define MAPSEL 0
DI constexpr int rowof(int j, int g, int w, int b) {
  return MAPSEL == 0 ? (b << 7 | w << 5 | g << 3 | j)
       : MAPSEL == 1 ? (b << 7 | (g >> 1) << 6 | w << 4 | (g & 1) << 3 | j)
                     : ((b >> 1) << 8 | (g >> 1) << 7 | (b & 1) << 6 | w << 4 | (g & 1) << 3 | j);
}

struct Blk { u32x4 x[8]; u32x4 e[4]; };

__global__ __launch_bounds__(256, 1) void linformer_kernel(const char* __restrict__ Q, const char* __restrict__ K, const char* __restrict__ V,
                                                            const char* __restrict__ E, const char* __restrict__ F, __hip_bfloat16* __restrict__ O) {
  __shared__ __attribute__((aligned(16))) float s_sc[NW][SQ][RR];
  __shared__ __attribute__((aligned(16))) float s_o[NW][DH][SQ];
  const int tid = threadIdx.x, lane = tid & 63, w = __builtin_amdgcn_readfirstlane(tid >> 6);
  const int g = lane >> 4, c = lane & 15;
  const int bh = blockIdx.x;
  const auto rk = __builtin_amdgcn_make_buffer_rsrc((void*)(K + (size_t)bh * HEAD_BYTES), 0, HEAD_BYTES, 0x00020000);
  const auto rv = __builtin_amdgcn_make_buffer_rsrc((void*)(V + (size_t)bh * HEAD_BYTES), 0, HEAD_BYTES, 0x00020000);
  const auto re = __builtin_amdgcn_make_buffer_rsrc((void*)E, 0, RR * SKV * 2, 0x00020000);
  const auto rf = __builtin_amdgcn_make_buffer_rsrc((void*)F, 0, RR * SKV * 2, 0x00020000);
  const auto rq = __builtin_amdgcn_make_buffer_rsrc((void*)(Q + (size_t)bh * SQ * DH * 2), 0, SQ * DH * 2, 0x00020000);
  const int xoff = rowof(0, g, 0, 0) * 256 + c * 16;
  const int eoff = c * (SKV * 2) + rowof(0, g, 0, 0) * 2;

  auto issue = [&](Blk& B, int i) {
    const int b = i % NB;
    const int so = rowof(0, 0, w, b);
#pragma unroll
    for (int j = 0; j < 8; ++j) B.x[j] = __builtin_amdgcn_raw_buffer_load_b128(i < NB ? rk : rv, xoff + j * 256, so * 256, 2);
#pragma unroll
    for (int rb = 0; rb < 4; ++rb) B.e[rb] = __builtin_amdgcn_raw_buffer_load_b128(i < NB ? re : rf, eoff, so * 2 + rb * 16 * SKV * 2, 0);
  };
  f32x4 acc[4][8];
  auto zero = [&]() {
#pragma unroll
    for (int rb = 0; rb < 4; ++rb)
#pragma unroll
      for (int t = 0; t < 8; ++t) acc[rb][t] = f32x4{0.f, 0.f, 0.f, 0.f};
  };
  auto consume = [&](const Blk& B, bool isK) {
#pragma unroll
    for (int t = 0; t < 8; ++t) {
      u32x4 T;
#pragma unroll
      for (int jp = 0; jp < 4; ++jp)
        T[jp] = __builtin_amdgcn_perm(B.x[2 * jp + 1][t >> 1], B.x[2 * jp][t >> 1], (t & 1) ? 0x07060302u : 0x05040100u);
#pragma unroll
      for (int rb = 0; rb < 4; ++rb) acc[rb][t] = isK ? mfma(T, B.e[rb], acc[rb][t]) : mfma(B.e[rb], T, acc[rb][t]);
    }
  };

  Blk buf[NBUF];
#pragma unroll
  for (int i = 0; i < NBUF; ++i) issue(buf[i], i);
  u32x4 qf[4];
#pragma unroll
  for (int i = 0; i < 4; ++i) qf[i] = __builtin_amdgcn_raw_buffer_load_b128(rq, (c & 3) * 256 + g * 64 + i * 16, 0, 0);
  __builtin_amdgcn_sched_barrier(0);
  zero();
#pragma unroll
  for (int i = 0; i < NB; ++i) {
    consume(buf[i % NBUF], true);
    __builtin_amdgcn_sched_barrier(0);
    issue(buf[i % NBUF], i + NBUF);
    __builtin_amdgcn_sched_barrier(0);
  }
#pragma unroll
  for (int rb = 0; rb < 4; ++rb) {
    f32x4 s = {0.f, 0.f, 0.f, 0.f};
#pragma unroll
    for (int i = 0; i < 4; ++i) {
      u32x4 bfr;
#pragma unroll
      for (int u = 0; u < 4; ++u) bfr[u] = pk(acc[rb][2 * u][i], acc[rb][2 * u + 1][i]);
      s = mfma(qf[i], bfr, s);
    }
    if (g == 0) {
#pragma unroll
      for (int qi = 0; qi < 4; ++qi) s_sc[w][qi][rb * 16 + c] = s[qi];
    }
  }
  __syncthreads();
  u32x4 pf[2];
  {
    const int qi = c & 3;
    f32x4 sv[4];
#pragma unroll
    for (int u = 0; u < 4; ++u) {
      f32x4 a = {0.f, 0.f, 0.f, 0.f};
#pragma unroll
      for (int ww = 0; ww < NW; ++ww) a += *(const f32x4*)&s_sc[ww][qi][16 * u + 4 * g];
      sv[u] = a * (0.08838834764831845f * 1.4426950408889634f);
    }
    float m = -INFINITY;
#pragma unroll
    for (int u = 0; u < 4; ++u)
#pragma unroll
      for (int i = 0; i < 4; ++i) m = fmaxf(m, sv[u][i]);
    m = fmaxf(m, __shfl_xor(m, 16));
    m = fmaxf(m, __shfl_xor(m, 32));
    float l = 0.f;
#pragma unroll
    for (int u = 0; u < 4; ++u)
#pragma unroll
      for (int i = 0; i < 4; ++i) { sv[u][i] = __builtin_amdgcn_exp2f(sv[u][i] - m); l += sv[u][i]; }
    l += __shfl_xor(l, 16);
    l += __shfl_xor(l, 32);
    const float inv = 1.0f / l;
#pragma unroll
    for (int s = 0; s < 2; ++s) {
      pf[s][0] = pk(sv[2 * s][0] * inv, sv[2 * s][1] * inv);
      pf[s][1] = pk(sv[2 * s][2] * inv, sv[2 * s][3] * inv);
      pf[s][2] = pk(sv[2 * s + 1][0] * inv, sv[2 * s + 1][1] * inv);
      pf[s][3] = pk(sv[2 * s + 1][2] * inv, sv[2 * s + 1][3] * inv);
    }
  }
  zero();
  __builtin_amdgcn_sched_barrier(0);
#pragma unroll
  for (int i = NB; i < 2 * NB; ++i) {
    consume(buf[i % NBUF], false);
    __builtin_amdgcn_sched_barrier(0);
    if (i + NBUF < 2 * NB) issue(buf[i % NBUF], i + NBUF);
    __builtin_amdgcn_sched_barrier(0);
  }
#pragma unroll
  for (int t = 0; t < 8; ++t) {
    f32x4 o = {0.f, 0.f, 0.f, 0.f};
#pragma unroll
    for (int s = 0; s < 2; ++s) {
      u32x4 bfr;
      bfr[0] = pk(acc[2 * s][t][0], acc[2 * s][t][1]);
      bfr[1] = pk(acc[2 * s][t][2], acc[2 * s][t][3]);
      bfr[2] = pk(acc[2 * s + 1][t][0], acc[2 * s + 1][t][1]);
      bfr[3] = pk(acc[2 * s + 1][t][2], acc[2 * s + 1][t][3]);
      o = mfma(pf[s], bfr, o);
    }
    if (g == 0) *(f32x4*)&s_o[w][8 * c + t][0] = o;
  }
  __syncthreads();
  {
    const int d2 = tid >> 2, qi = tid & 3;
    float a0 = 0.f, a1 = 0.f;
#pragma unroll
    for (int ww = 0; ww < NW; ++ww) { a0 += s_o[ww][2 * d2][qi]; a1 += s_o[ww][2 * d2 + 1][qi]; }
    ((unsigned*)O)[(size_t)bh * SQ * DH / 2 + qi * DH / 2 + d2] = pk(a0, a1);
  }
}

extern "C" void kernel_launch(void* const* d_in, const int* in_sizes, int n_in, void* d_out, int out_size, void* d_ws, size_t ws_size,
                              hipStream_t stream) {
  linformer_kernel<<<256, 256, 0, stream>>>((const char*)d_in[0], (const char*)d_in[1], (const char*)d_in[2], (const char*)d_in[3],
                                           (const char*)d_in[4], (__hip_bfloat16*)d_out);
}
```

```cpp
#define DEPTH 4
#include <hip/hip_runtime.h>
#include <hip/hip_bf16.h>
#include <cstdio>

typedef unsigned u32x4 __attribute__((ext_vector_type(4)));
typedef unsigned u32x2 __attribute__((ext_vector_type(2)));
typedef float f32x4 __attribute__((ext_vector_type(4)));
typedef float f32x16 __attribute__((ext_vector_type(16)));
typedef short s16x4 __attribute__((ext_vector_type(4)));
typedef __bf16 bf16x8 __attribute__((ext_vector_type(8)));
typedef __bf16 bf16x2 __attribute__((ext_vector_type(2)));
#define DI __device__ __forceinline__
#define LAS __attribute__((address_space(3)))

#define RN 64
#define ENT ""
#define ROT 0
constexpr int SKV = 2048, DH = 128, RR = 64, SQ = 4, NW = 4;
constexpr int HEAD_BYTES = SKV * DH * 2;
constexpr int NR = SKV / RN;
constexpr int XB = RN * 256;
constexpr int EL = RN * 2;
constexpr int EB = RR * EL;
constexpr int SLOT = XB + EB;
constexpr int XI = XB / 1024 / NW;
constexpr int EI = EB / 1024 / NW;
constexpr int NI = XI + EI;
constexpr int KS = RN / 16;
constexpr int Q_OFF = DEPTH * SLOT, SC_OFF = Q_OFF + 1024, ST_OFF = SC_OFF + NW * SQ * RR * 4, PF_OFF = ST_OFF + 1024, LDS_BYTES = PF_OFF + 4096;
static_assert(LDS_BYTES <= 163840, "LDS");
static_assert((DEPTH - 2) * NI <= 63, "vmcnt");

DI f32x16 mfma(u32x4 a, u32x4 b, f32x16 c) {
  return __builtin_amdgcn_mfma_f32_32x32x16_bf16(__builtin_bit_cast(bf16x8, a), __builtin_bit_cast(bf16x8, b), c, 0, 0, 0);
}
DI unsigned pk(float lo, float hi) {
  bf16x2 v = {(__bf16)lo, (__bf16)hi};
  return __builtin_bit_cast(unsigned, v);
}
DI u32x2 trd(unsigned a) {
  return __builtin_bit_cast(u32x2, __builtin_amdgcn_ds_read_tr16_b64_v4i16((LAS s16x4*)a));
}
DI u32x4 srd(const char* p, unsigned bytes) {
  const unsigned long long a = (unsigned long long)p;
  return u32x4{(unsigned)a, (unsigned)(a >> 32) & 0xffffu, bytes, 0x00020000u};
}
template <int N> DI void wait_vm() { asm volatile("s_waitcnt vmcnt(%0)" ::"n"(N) : "memory"); }

#define STAMP_ARG
#define STAMP_AT(i)
#define PF 0
__global__ __launch_bounds__(256, 1) void linformer_kernel(const char* __restrict__ Q, const char* __restrict__ K, const char* __restrict__ V,
                                                            const char* __restrict__ E, const char* __restrict__ F, __hip_bfloat16* __restrict__ O STAMP_ARG) {
  __shared__ __attribute__((aligned(1024))) char lds[LDS_BYTES];
  const int tid = threadIdx.x, lane = tid & 63, w = __builtin_amdgcn_readfirstlane(tid >> 6);
  const int bh = blockIdx.x;
  const unsigned lb = (unsigned)(size_t)(LAS char*)lds;
  const char* kp = K + (size_t)bh * HEAD_BYTES;
  const char* vp = V + (size_t)bh * HEAD_BYTES;
  const int rot = ROT == 0 ? 0 : ROT == 1 ? ((bh >> 3) & 7) * (NR / 8) : ROT == 2 ? ((bh >> 3) & 3) * (NR / 4) : ROT == 3 ? ((bh >> 3) & 1) * (NR / 2) : ((bh >> 3) & 15) * (NR / 16);
  STAMP_AT(0);
  const unsigned long long tt0 = __builtin_amdgcn_s_memrealtime();

  unsigned xv[4], ev[EI];
  {
    const int g = lane >> 4, c = lane & 15;
#pragma unroll
    for (int u = 0; u < 4; ++u) xv[u] = (unsigned)(w * XI * 1024 + g * 256 + ((c ^ ((g << 2) | ((w * XI + u) & 3))) << 4));
    constexpr int CPR = EL / 16, RPI = 64 / CPR;
#pragma unroll
    for (int u = 0; u < EI; ++u) {
      const int row = (w * EI + u) * RPI + lane / CPR, pos = lane % CPR;
      const int sw = EL == 256 ? (row & 15) : ((row >> 1) & 7);
      ev[u] = (unsigned)(row * (SKV * 2) + ((pos ^ sw) << 4) - u * 1024);
    }
  }
  auto issue = [&](int j, int slot) {
    const bool isv = j >= NR;
    const unsigned live = j < 2 * NR ? 1u : 0u;
    const u32x4 rx = srd(isv ? vp : kp, live * HEAD_BYTES), re = srd(isv ? F : E, live * (RR * SKV * 2));
    const int jj = (j + rot) & (NR - 1);
    const unsigned sx = jj * XB, se = jj * EL;
    const unsigned mx = lb + slot * SLOT + w * XI * 1024, me = lb + slot * SLOT + XB + w * EI * 1024;
    asm volatile("s_nop 4\n\ts_mov_b32 m0, %0\n\ts_nop 0\n\t"
                 "buffer_load_dwordx4 %1, %5, %6 offen nt lds\n\t"
                 "buffer_load_dwordx4 %2, %5, %6 offen offset:1024 nt lds\n\t"
                 "buffer_load_dwordx4 %3, %5, %6 offen offset:2048 nt lds\n\t"
                 "buffer_load_dwordx4 %4, %5, %6 offen offset:3072 nt lds"
                 ::"s"(mx), "v"(xv[0]), "v"(xv[1]), "v"(xv[2]), "v"(xv[3]), "s"(rx), "s"(sx) : "memory");
    if constexpr (XI == 8)
      asm volatile("s_mov_b32 m0, %0\n\ts_nop 0\n\t"
                   "buffer_load_dwordx4 %1, %5, %6 offen nt lds\n\t"
                   "buffer_load_dwordx4 %2, %5, %6 offen offset:1024 nt lds\n\t"
                   "buffer_load_dwordx4 %3, %5, %6 offen offset:2048 nt lds\n\t"
                   "buffer_load_dwordx4 %4, %5, %6 offen offset:3072 nt lds"
                   ::"s"(mx + 4096), "v"(xv[0]), "v"(xv[1]), "v"(xv[2]), "v"(xv[3]), "s"(rx), "s"(sx + 4096) : "memory");
    if constexpr (EI == 2)
      asm volatile("s_mov_b32 m0, %0\n\ts_nop 0\n\t"
                   "buffer_load_dwordx4 %1, %3, %4 offen " ENT " lds\n\t"
                   "buffer_load_dwordx4 %2, %3, %4 offen offset:1024 " ENT " lds"
                   ::"s"(me), "v"(ev[0]), "v"(ev[1]), "s"(re), "s"(se) : "memory");
    else
      asm volatile("s_mov_b32 m0, %0\n\ts_nop 0\n\t"
                   "buffer_load_dwordx4 %1, %5, %6 offen " ENT " lds\n\t"
                   "buffer_load_dwordx4 %2, %5, %6 offen offset:1024 " ENT " lds\n\t"
                   "buffer_load_dwordx4 %3, %5, %6 offen offset:2048 " ENT " lds\n\t"
                   "buffer_load_dwordx4 %4, %5, %6 offen offset:3072 " ENT " lds"
                   ::"s"(me), "v"(ev[0]), "v"(ev[1]), "v"(ev[EI - 2]), "v"(ev[EI - 1]), "s"(re), "s"(se) : "memory");
  };
  STAMP_AT(98);
  issue(0, 0);
  STAMP_AT(99);
  if (w == 0) {
    const u32x4 rq = srd(Q + (size_t)bh * (SQ * DH * 2), SQ * DH * 2);
    asm volatile("s_nop 4\n\ts_mov_b32 m0, %0\n\ts_nop 0\n\tbuffer_load_dwordx4 %1, %2, 0 offen lds" ::"s"(lb + Q_OFF), "v"(lane * 16), "s"(rq) : "memory");
  }
  if (PF) {
    const u32x4 rp = srd(w < 2 ? E : F, RR * SKV * 2);
    const unsigned so = (((bh >> 3) & 31) * 2 + (w & 1)) * 4096;
    asm volatile("s_nop 4\n\ts_mov_b32 m0, %0\n\ts_nop 0\n\t"
                 "buffer_load_dwordx4 %1, %2, %3 offen lds\n\t"
                 "buffer_load_dwordx4 %1, %2, %3 offen offset:1024 lds\n\t"
                 "buffer_load_dwordx4 %1, %2, %3 offen offset:2048 lds\n\t"
                 "buffer_load_dwordx4 %1, %2, %3 offen offset:3072 lds"
                 ::"s"(lb + PF_OFF), "v"(lane * 16), "s"(rp), "s"(so) : "memory");
  }
#pragma unroll
  for (int j = 1; j < DEPTH - 1; ++j) issue(j, j);

  const int h = lane >> 5, m = lane & 31;
  unsigned xr[2], er;
  {
    const int blk = (lane >> 4) & 1, q = (lane & 15) >> 2, p = lane & 3;
#pragma unroll
    for (int t = 0; t < 2; ++t) xr[t] = lb + 256 * (8 * h + 4 * t + q) + 16 * (((w ^ q) << 2) | ((2 * blk + (p >> 1)) ^ (2 * h + t))) + 8 * (p & 1);
    const int sw = EL == 256 ? (m & 15) : ((m >> 1) & 7);
    er = lb + XB + m * EL + 16 * (h ^ sw);
  }
  f32x16 acc[2];
  int slot = 0;
  auto round = [&](int i, bool isK) {
    wait_vm<(DEPTH - 2) * NI>();
    __builtin_amdgcn_s_barrier();
    asm volatile("" ::: "memory");
    STAMP_AT(1 + i);
    issue(i + DEPTH - 1, slot == 0 ? DEPTH - 1 : slot - 1);
    const unsigned so = slot * SLOT;
    const unsigned xa0 = xr[0] + so, xa1 = xr[1] + so, ea = er + so;
    u32x4 xf[KS], e0[KS], e1[KS];
#pragma unroll
    for (int ks = 0; ks < KS; ++ks) {
      const u32x2 x0 = trd(xa0 + 4096 * ks), x1 = trd(xa1 + 4096 * ks);
      xf[ks] = u32x4{x0[0], x0[1], x1[0], x1[1]};
      e0[ks] = *(const LAS u32x4*)(ea ^ (32 * ks));
      e1[ks] = *(const LAS u32x4*)((ea ^ (32 * ks)) + 32 * EL);
    }
    __builtin_amdgcn_sched_barrier(0);
#pragma unroll
    for (int ks = 0; ks < KS; ++ks) {
      acc[0] = isK ? mfma(xf[ks], e0[ks], acc[0]) : mfma(e0[ks], xf[ks], acc[0]);
      acc[1] = isK ? mfma(xf[ks], e1[ks], acc[1]) : mfma(e1[ks], xf[ks], acc[1]);
    }
    __builtin_amdgcn_sched_barrier(0);
    asm volatile("s_waitcnt lgkmcnt(0)" ::: "memory");
    slot = slot == DEPTH - 1 ? 0 : slot + 1;
  };
  auto cvt8 = [&](const f32x16& x, int s) {
    return u32x4{pk(x[8 * s], x[8 * s + 1]), pk(x[8 * s + 2], x[8 * s + 3]), pk(x[8 * s + 4], x[8 * s + 5]), pk(x[8 * s + 6], x[8 * s + 7])};
  };
  const f32x16 zero16 = {0.f, 0.f, 0.f, 0.f, 0.f, 0.f, 0.f, 0.f, 0.f, 0.f, 0.f, 0.f, 0.f, 0.f, 0.f, 0.f};

  acc[0] = zero16; acc[1] = zero16;
#pragma unroll 1
  for (int i = 0; i < NR; ++i) round(i, true);

  {
    LAS float* sc = (LAS float*)(lb + SC_OFF);
    u32x4 qf[2];
#pragma unroll
    for (int s = 0; s < 2; ++s) {
      const unsigned qa = lb + Q_OFF + (m & 3) * 256 + (32 * w + 16 * s + 4 * h) * 2;
      const u32x2 a = *(const LAS u32x2*)qa, b = *(const LAS u32x2*)(qa + 16);
      qf[s] = u32x4{a[0], a[1], b[0], b[1]};
    }
#pragma unroll
    for (int rb = 0; rb < 2; ++rb) {
      f32x16 s = mfma(qf[0], cvt8(acc[rb], 0), zero16);
      s = mfma(qf[1], cvt8(acc[rb], 1), s);
      if (h == 0) {
#pragma unroll
        for (int qi = 0; qi < 4; ++qi) sc[(w * SQ + qi) * RR + 32 * rb + m] = s[qi];
      }
    }
    asm volatile("s_waitcnt lgkmcnt(0)" ::: "memory");
  }
  acc[0] = zero16; acc[1] = zero16;
  round(NR, false);
  u32x4 pf[4];
  {
    const LAS float* sc = (const LAS float*)(lb + SC_OFF) + (m & 3) * RR + 4 * h;
    f32x4 sv[8];
#pragma unroll
    for (int u = 0; u < 8; ++u) {
      f32x4 a = *(const LAS f32x4*)(sc + 8 * u);
#pragma unroll
      for (int ww = 1; ww < NW; ++ww) a += *(const LAS f32x4*)(sc + ww * SQ * RR + 8 * u);
      sv[u] = a * (0.08838834764831845f * 1.4426950408889634f);
    }
    float mx = -INFINITY;
#pragma unroll
    for (int u = 0; u < 8; ++u)
#pragma unroll
      for (int i = 0; i < 4; ++i) mx = fmaxf(mx, sv[u][i]);
    mx = fmaxf(mx, __shfl_xor(mx, 32));
    float l = 0.f;
#pragma unroll
    for (int u = 0; u < 8; ++u)
#pragma unroll
      for (int i = 0; i < 4; ++i) { sv[u][i] = __builtin_amdgcn_exp2f(sv[u][i] - mx); l += sv[u][i]; }
    l += __shfl_xor(l, 32);
    const float inv = 1.0f / l;
#pragma unroll
    for (int s = 0; s < 4; ++s)
      pf[s] = u32x4{pk(sv[2 * s][0] * inv, sv[2 * s][1] * inv), pk(sv[2 * s][2] * inv, sv[2 * s][3] * inv),
                    pk(sv[2 * s + 1][0] * inv, sv[2 * s + 1][1] * inv), pk(sv[2 * s + 1][2] * inv, sv[2 * s + 1][3] * inv)};
  }
#pragma unroll 1
  for (int i = NR + 1; i < 2 * NR; ++i) round(i, false);

  {
    f32x16 o0 = mfma(cvt8(acc[0], 0), pf[0], zero16);
    f32x16 o1 = mfma(cvt8(acc[1], 0), pf[2], zero16);
    o0 = mfma(cvt8(acc[0], 1), pf[1], o0);
    o1 = mfma(cvt8(acc[1], 1), pf[3], o1);
    if (m < 4) {
      char* op = (char*)O + (size_t)bh * (SQ * DH * 2) + m * (DH * 2) + (32 * w + 4 * h) * 2;
#pragma unroll
      for (int gq = 0; gq < 4; ++gq)
        *(u32x2*)(op + 16 * gq) = u32x2{pk(o0[4 * gq] + o1[4 * gq], o0[4 * gq + 1] + o1[4 * gq + 1]), pk(o0[4 * gq + 2] + o1[4 * gq + 2], o0[4 * gq + 3] + o1[4 * gq + 3])};
    }
  }
  while ((long long)(__builtin_amdgcn_s_memrealtime() - tt0) < 6000) __builtin_amdgcn_s_sleep(1);
}

extern "C" void kernel_launch(void* const* d_in, const int* in_sizes, int n_in, void* d_out, int out_size, void* d_ws, size_t ws_size,
                              hipStream_t stream) {
  fprintf(stderr, "ptrs q %p k %p v %p e %p f %p out %p ws %p\n", d_in[0], d_in[1], d_in[2], d_in[3], d_in[4], d_out, d_ws);
  linformer_kernel<<<256, 256, 0, stream>>>((const char*)d_in[0], (const char*)d_in[1], (const char*)d_in[2], (const char*)d_in[3],
                                           (const char*)d_in[4], (__hip_bfloat16*)d_out
  );
}
```

```cpp
#define DEPTH 4
#include <hip/hip_runtime.h>
#include <hip/hip_bf16.h>
#include <cstdio>

typedef unsigned u32x4 __attribute__((ext_vector_type(4)));
typedef unsigned u32x2 __attribute__((ext_vector_type(2)));
typedef float f32x4 __attribute__((ext_vector_type(4)));
typedef float f32x16 __attribute__((ext_vector_type(16)));
typedef short s16x4 __attribute__((ext_vector_type(4)));
typedef __bf16 bf16x8 __attribute__((ext_vector_type(8)));
typedef __bf16 bf16x2 __attribute__((ext_vector_type(2)));
#define DI __device__ __forceinline__
#define LAS __attribute__((address_space(3)))

#define RN 64
#define ENT "sc0 sc1"
#define ROT 0
constexpr int SKV = 2048, DH = 128, RR = 64, SQ = 4, NW = 4;
constexpr int HEAD_BYTES = SKV * DH * 2;
constexpr int NR = SKV / RN;
constexpr int XB = RN * 256;
constexpr int EL = RN * 2;
constexpr int EB = RR * EL;
constexpr int SLOT = XB + EB;
constexpr int XI = XB / 1024 / NW;
constexpr int EI = EB / 1024 / NW;
constexpr int NI = XI + EI;
constexpr int KS = RN / 16;
constexpr int Q_OFF = DEPTH * SLOT, SC_OFF = Q_OFF + 1024, ST_OFF = SC_OFF + NW * SQ * RR * 4, PF_OFF = ST_OFF + 1024, LDS_BYTES = PF_OFF + 4096;
static_assert(LDS_BYTES <= 163840, "LDS");
static_assert((DEPTH - 2) * NI <= 63, "vmcnt");

DI f32x16 mfma(u32x4 a, u32x4 b, f32x16 c) {
  return __builtin_amdgcn_mfma_f32_32x32x16_bf16(__builtin_bit_cast(bf16x8, a), __builtin_bit_cast(bf16x8, b), c, 0, 0, 0);
}
DI unsigned pk(float lo, float hi) {
  bf16x2 v = {(__bf16)lo, (__bf16)hi};
  return __builtin_bit_cast(unsigned, v);
}
DI u32x2 trd(unsigned a) {
  return __builtin_bit_cast(u32x2, __builtin_amdgcn_ds_read_tr16_b64_v4i16((LAS s16x4*)a));
}
DI u32x4 srd(const char* p, unsigned bytes) {
  const unsigned long long a = (unsigned long long)p;
  return u32x4{(unsigned)a, (unsigned)(a >> 32) & 0xffffu, bytes, 0x00020000u};
}
template <int N> DI void wait_vm() { asm volatile("s_waitcnt vmcnt(%0)" ::"n"(N) : "memory"); }

#define STAMP_ARG
#define STAMP_AT(i)
#define PF 0
__global__ __launch_bounds__(256, 1) void linformer_kernel(const char* __restrict__ Q, const char* __restrict__ K, const char* __restrict__ V,
                                                            const char* __restrict__ E, const char* __restrict__ F, __hip_bfloat16* __restrict__ O STAMP_ARG) {
  __shared__ __attribute__((aligned(1024))) char lds[LDS_BYTES];
  const int tid = threadIdx.x, lane = tid & 63, w = __builtin_amdgcn_readfirstlane(tid >> 6);
  const int bh = blockIdx.x;
  const unsigned lb = (unsigned)(size_t)(LAS char*)lds;
  const char* kp = K + (size_t)bh * HEAD_BYTES;
  const char* vp = V + (size_t)bh * HEAD_BYTES;
  const int rot = ROT == 0 ? 0 : ROT == 1 ? ((bh >> 3) & 7) * (NR / 8) : ROT == 2 ? ((bh >> 3) & 3) * (NR / 4) : ROT == 3 ? ((bh >> 3) & 1) * (NR / 2) : ((bh >> 3) & 15) * (NR / 16);
  STAMP_AT(0);
  const unsigned long long tt0 = __builtin_amdgcn_s_memrealtime();

  unsigned xv[4], ev[EI];
  {
    const int g = lane >> 4, c = lane & 15;
#pragma unroll
    for (int u = 0; u < 4; ++u) xv[u] = (unsigned)(w * XI * 1024 + g * 256 + ((c ^ ((g << 2) | ((w * XI + u) & 3))) << 4));
    constexpr int CPR = EL / 16, RPI = 64 / CPR;
#pragma unroll
    for (int u = 0; u < EI; ++u) {
      const int row = (w * EI + u) * RPI + lane / CPR, pos = lane % CPR;
      const int sw = EL == 256 ? (row & 15) : ((row >> 1) & 7);
      ev[u] = (unsigned)(row * (SKV * 2) + ((pos ^ sw) << 4) - u * 1024);
    }
  }
  auto issue = [&](int j, int slot) {
    const bool isv = j >= NR;
    const unsigned live = j < 2 * NR ? 1u : 0u;
    const u32x4 rx = srd(isv ? vp : kp, live * HEAD_BYTES), re = srd(isv ? F : E, live * (RR * SKV * 2));
    const int jj = (j + rot) & (NR - 1);
    const unsigned sx = jj * XB, se = jj * EL;
    const unsigned mx = lb + slot * SLOT + w * XI * 1024, me = lb + slot * SLOT + XB + w * EI * 1024;
    asm volatile("s_nop 4\n\ts_mov_b32 m0, %0\n\ts_nop 0\n\t"
                 "buffer_load_dwordx4 %1, %5, %6 offen sc0 nt sc1 lds\n\t"
                 "buffer_load_dwordx4 %2, %5, %6 offen offset:1024 sc0 nt sc1 lds\n\t"
                 "buffer_load_dwordx4 %3, %5, %6 offen offset:2048 sc0 nt sc1 lds\n\t"
                 "buffer_load_dwordx4 %4, %5, %6 offen offset:3072 sc0 nt sc1 lds"
                 ::"s"(mx), "v"(xv[0]), "v"(xv[1]), "v"(xv[2]), "v"(xv[3]), "s"(rx), "s"(sx) : "memory");
    if constexpr (XI == 8)
      asm volatile("s_mov_b32 m0, %0\n\ts_nop 0\n\t"
                   "buffer_load_dwordx4 %1, %5, %6 offen sc0 nt sc1 lds\n\t"
                   "buffer_load_dwordx4 %2, %5, %6 offen offset:1024 sc0 nt sc1 lds\n\t"
                   "buffer_load_dwordx4 %3, %5, %6 offen offset:2048 sc0 nt sc1 lds\n\t"
                   "buffer_load_dwordx4 %4, %5, %6 offen offset:3072 sc0 nt sc1 lds"
                   ::"s"(mx + 4096), "v"(xv[0]), "v"(xv[1]), "v"(xv[2]), "v"(xv[3]), "s"(rx), "s"(sx + 4096) : "memory");
    if constexpr (EI == 2)
      asm volatile("s_mov_b32 m0, %0\n\ts_nop 0\n\t"
                   "buffer_load_dwordx4 %1, %3, %4 offen " ENT " lds\n\t"
                   "buffer_load_dwordx4 %2, %3, %4 offen offset:1024 " ENT " lds"
                   ::"s"(me), "v"(ev[0]), "v"(ev[1]), "s"(re), "s"(se) : "memory");
    else
      asm volatile("s_mov_b32 m0, %0\n\ts_nop 0\n\t"
                   "buffer_load_dwordx4 %1, %5, %6 offen " ENT " lds\n\t"
                   "buffer_load_dwordx4 %2, %5, %6 offen offset:1024 " ENT " lds\n\t"
                   "buffer_load_dwordx4 %3, %5, %6 offen offset:2048 " ENT " lds\n\t"
                   "buffer_load_dwordx4 %4, %5, %6 offen offset:3072 " ENT " lds"
                   ::"s"(me), "v"(ev[0]), "v"(ev[1]), "v"(ev[EI - 2]), "v"(ev[EI - 1]), "s"(re), "s"(se) : "memory");
  };
  STAMP_AT(98);
  issue(0, 0);
  STAMP_AT(99);
  if (w == 0) {
    const u32x4 rq = srd(Q + (size_t)bh * (SQ * DH * 2), SQ * DH * 2);
    asm volatile("s_nop 4\n\ts_mov_b32 m0, %0\n\ts_nop 0\n\tbuffer_load_dwordx4 %1, %2, 0 offen lds" ::"s"(lb + Q_OFF), "v"(lane * 16), "s"(rq) : "memory");
  }
  if (PF) {
    const u32x4 rp = srd(w < 2 ? E : F, RR * SKV * 2);
    const unsigned so = (((bh >> 3) & 31) * 2 + (w & 1)) * 4096;
    asm volatile("s_nop 4\n\ts_mov_b32 m0, %0\n\ts_nop 0\n\t"
                 "buffer_load_dwordx4 %1, %2, %3 offen lds\n\t"
                 "buffer_load_dwordx4 %1, %2, %3 offen offset:1024 lds\n\t"
                 "buffer_load_dwordx4 %1, %2, %3 offen offset:2048 lds\n\t"
                 "buffer_load_dwordx4 %1, %2, %3 offen offset:3072 lds"
                 ::"s"(lb + PF_OFF), "v"(lane * 16), "s"(rp), "s"(so) : "memory");
  }
#pragma unroll
  for (int j = 1; j < DEPTH - 1; ++j) issue(j, j);

  const int h = lane >> 5, m = lane & 31;
  unsigned xr[2], er;
  {
    const int blk = (lane >> 4) & 1, q = (lane & 15) >> 2, p = lane & 3;
#pragma unroll
    for (int t = 0; t < 2; ++t) xr[t] = lb + 256 * (8 * h + 4 * t + q) + 16 * (((w ^ q) << 2) | ((2 * blk + (p >> 1)) ^ (2 * h + t))) + 8 * (p & 1);
    const int sw = EL == 256 ? (m & 15) : ((m >> 1) & 7);
    er = lb + XB + m * EL + 16 * (h ^ sw);
  }
  f32x16 acc[2];
  int slot = 0;
  auto round = [&](int i, bool isK) {
    wait_vm<(DEPTH - 2) * NI>();
    __builtin_amdgcn_s_barrier();
    asm volatile("" ::: "memory");
    STAMP_AT(1 + i);
    issue(i + DEPTH - 1, slot == 0 ? DEPTH - 1 : slot - 1);
    const unsigned so = slot * SLOT;
    const unsigned xa0 = xr[0] + so, xa1 = xr[1] + so, ea = er + so;
    u32x4 xf[KS], e0[KS], e1[KS];
#pragma unroll
    for (int ks = 0; ks < KS; ++ks) {
      const u32x2 x0 = trd(xa0 + 4096 * ks), x1 = trd(xa1 + 4096 * ks);
      xf[ks] = u32x4{x0[0], x0[1], x1[0], x1[1]};
      e0[ks] = *(const LAS u32x4*)(ea ^ (32 * ks));
      e1[ks] = *(const LAS u32x4*)((ea ^ (32 * ks)) + 32 * EL);
    }
    __builtin_amdgcn_sched_barrier(0);
#pragma unroll
    for (int ks = 0; ks < KS; ++ks) {
      acc[0] = isK ? mfma(xf[ks], e0[ks], acc[0]) : mfma(e0[ks], xf[ks], acc[0]);
      acc[1] = isK ? mfma(xf[ks], e1[ks], acc[1]) : mfma(e1[ks], xf[ks], acc[1]);
    }
    __builtin_amdgcn_sched_barrier(0);
    asm volatile("s_waitcnt lgkmcnt(0)" ::: "memory");
    slot = slot == DEPTH - 1 ? 0 : slot + 1;
  };
  auto cvt8 = [&](const f32x16& x, int s) {
    return u32x4{pk(x[8 * s], x[8 * s + 1]), pk(x[8 * s + 2], x[8 * s + 3]), pk(x[8 * s + 4], x[8 * s + 5]), pk(x[8 * s + 6], x[8 * s + 7])};
  };
  const f32x16 zero16 = {0.f, 0.f, 0.f, 0.f, 0.f, 0.f, 0.f, 0.f, 0.f, 0.f, 0.f, 0.f, 0.f, 0.f, 0.f, 0.f};

  acc[0] = zero16; acc[1] = zero16;
#pragma unroll 1
  for (int i = 0; i < NR; ++i) round(i, true);

  {
    LAS float* sc = (LAS float*)(lb + SC_OFF);
    u32x4 qf[2];
#pragma unroll
    for (int s = 0; s < 2; ++s) {
      const unsigned qa = lb + Q_OFF + (m & 3) * 256 + (32 * w + 16 * s + 4 * h) * 2;
      const u32x2 a = *(const LAS u32x2*)qa, b = *(const LAS u32x2*)(qa + 16);
      qf[s] = u32x4{a[0], a[1], b[0], b[1]};
    }
#pragma unroll
    for (int rb = 0; rb < 2; ++rb) {
      f32x16 s = mfma(qf[0], cvt8(acc[rb], 0), zero16);
      s = mfma(qf[1], cvt8(acc[rb], 1), s);
      if (h == 0) {
#pragma unroll
        for (int qi = 0; qi < 4; ++qi) sc[(w * SQ + qi) * RR + 32 * rb + m] = s[qi];
      }
    }
    asm volatile("s_waitcnt lgkmcnt(0)" ::: "memory");
  }
  acc[0] = zero16; acc[1] = zero16;
  round(NR, false);
  u32x4 pf[4];
  {
    const LAS float* sc = (const LAS float*)(lb + SC_OFF) + (m & 3) * RR + 4 * h;
    f32x4 sv[8];
#pragma unroll
    for (int u = 0; u < 8; ++u) {
      f32x4 a = *(const LAS f32x4*)(sc + 8 * u);
#pragma unroll
      for (int ww = 1; ww < NW; ++ww) a += *(const LAS f32x4*)(sc + ww * SQ * RR + 8 * u);
      sv[u] = a * (0.08838834764831845f * 1.4426950408889634f);
    }
    float mx = -INFINITY;
#pragma unroll
    for (int u = 0; u < 8; ++u)
#pragma unroll
      for (int i = 0; i < 4; ++i) mx = fmaxf(mx, sv[u][i]);
    mx = fmaxf(mx, __shfl_xor(mx, 32));
    float l = 0.f;
#pragma unroll
    for (int u = 0; u < 8; ++u)
#pragma unroll
      for (int i = 0; i < 4; ++i) { sv[u][i] = __builtin_amdgcn_exp2f(sv[u][i] - mx); l += sv[u][i]; }
    l += __shfl_xor(l, 32);
    const float inv = 1.0f / l;
#pragma unroll
    for (int s = 0; s < 4; ++s)
      pf[s] = u32x4{pk(sv[2 * s][0] * inv, sv[2 * s][1] * inv), pk(sv[2 * s][2] * inv, sv[2 * s][3] * inv),
                    pk(sv[2 * s + 1][0] * inv, sv[2 * s + 1][1] * inv), pk(sv[2 * s + 1][2] * inv, sv[2 * s + 1][3] * inv)};
  }
#pragma unroll 1
  for (int i = NR + 1; i < 2 * NR; ++i) round(i, false);

  {
    f32x16 o0 = mfma(cvt8(acc[0], 0), pf[0], zero16);
    f32x16 o1 = mfma(cvt8(acc[1], 0), pf[2], zero16);
    o0 = mfma(cvt8(acc[0], 1), pf[1], o0);
    o1 = mfma(cvt8(acc[1], 1), pf[3], o1);
    if (m < 4) {
      char* op = (char*)O + (size_t)bh * (SQ * DH * 2) + m * (DH * 2) + (32 * w + 4 * h) * 2;
#pragma unroll
      for (int gq = 0; gq < 4; ++gq)
      { const u32x2 val = u32x2{pk(o0[4 * gq] + o1[4 * gq], o0[4 * gq + 1] + o1[4 * gq + 1]), pk(o0[4 * gq + 2] + o1[4 * gq + 2], o0[4 * gq + 3] + o1[4 * gq + 3])};
        asm volatile("global_store_dwordx2 %0, %1, off sc0 sc1" :: "v"(op + 16 * gq), "v"(val) : "memory"); }
    }
  }
  while ((long long)(__builtin_amdgcn_s_memrealtime() - tt0) < 6000) __builtin_amdgcn_s_sleep(1);
}

extern "C" void kernel_launch(void* const* d_in, const int* in_sizes, int n_in, void* d_out, int out_size, void* d_ws, size_t ws_size,
                              hipStream_t stream) {
  linformer_kernel<<<256, 256, 0, stream>>>((const char*)d_in[0], (const char*)d_in[1], (const char*)d_in[2], (const char*)d_in[3],
                                           (const char*)d_in[4], (__hip_bfloat16*)d_out
  );
}
```

```cpp
#define DEPTH 4
#include <hip/hip_runtime.h>
#include <hip/hip_bf16.h>
#include <cstdio>

typedef unsigned u32x4 __attribute__((ext_vector_type(4)));
typedef unsigned u32x2 __attribute__((ext_vector_type(2)));
typedef float f32x4 __attribute__((ext_vector_type(4)));
typedef float f32x16 __attribute__((ext_vector_type(16)));
typedef short s16x4 __attribute__((ext_vector_type(4)));
typedef __bf16 bf16x8 __attribute__((ext_vector_type(8)));
typedef __bf16 bf16x2 __attribute__((ext_vector_type(2)));
#define DI __device__ __forceinline__
#define LAS __attribute__((address_space(3)))

#define RN 64
#define ENT ""
#define ROT 0
constexpr int SKV = 2048, DH = 128, RR = 64, SQ = 4, NW = 4;
constexpr int HEAD_BYTES = SKV * DH * 2;
constexpr int NR = SKV / RN;
constexpr int XB = RN * 256;
constexpr int EL = RN * 2;
constexpr int EB = RR * EL;
constexpr int SLOT = XB + EB;
constexpr int XI = XB / 1024 / NW;
constexpr int EI = EB / 1024 / NW;
constexpr int NI = XI + EI;
constexpr int KS = RN / 16;
constexpr int Q_OFF = DEPTH * SLOT, SC_OFF = Q_OFF + 1024, ST_OFF = SC_OFF + NW * SQ * RR * 4, PF_OFF = ST_OFF + 1024, LDS_BYTES = PF_OFF + 4096;
static_assert(LDS_BYTES <= 163840, "LDS");
static_assert((DEPTH - 2) * NI <= 63, "vmcnt");

DI f32x16 mfma(u32x4 a, u32x4 b, f32x16 c) {
  return __builtin_amdgcn_mfma_f32_32x32x16_bf16(__builtin_bit_cast(bf16x8, a), __builtin_bit_cast(bf16x8, b), c, 0, 0, 0);
}
DI unsigned pk(float lo, float hi) {
  bf16x2 v = {(__bf16)lo, (__bf16)hi};
  return __builtin_bit_cast(unsigned, v);
}
DI u32x2 trd(unsigned a) {
  return __builtin_bit_cast(u32x2, __builtin_amdgcn_ds_read_tr16_b64_v4i16((LAS s16x4*)a));
}
DI u32x4 srd(const char* p, unsigned bytes) {
  const unsigned long long a = (unsigned long long)p;
  return u32x4{(unsigned)a, (unsigned)(a >> 32) & 0xffffu, bytes, 0x00020000u};
}
template <int N> DI void wait_vm() { asm volatile("s_waitcnt vmcnt(%0)" ::"n"(N) : "memory"); }

#define STAMP_ARG
#define STAMP_AT(i)
#define PF 0
__global__ __launch_bounds__(256, 1) void linformer_kernel(const char* __restrict__ Q, const char* __restrict__ K, const char* __restrict__ V,
                                                            const char* __restrict__ E, const char* __restrict__ F, __hip_bfloat16* __restrict__ O STAMP_ARG) {
  __shared__ __attribute__((aligned(1024))) char lds[LDS_BYTES];
  unsigned long long tt0; asm volatile("s_memrealtime %0" : "=s"(tt0));
  __builtin_amdgcn_sched_barrier(0);
  const int tid = threadIdx.x, lane = tid & 63, w = __builtin_amdgcn_readfirstlane(tid >> 6);
  const int bh = blockIdx.x;
  const unsigned lb = (unsigned)(size_t)(LAS char*)lds;
  const char* kp = K + (size_t)bh * HEAD_BYTES;
  const char* vp = V + (size_t)bh * HEAD_BYTES;
  const int rot = ROT == 0 ? 0 : ROT == 1 ? ((bh >> 3) & 7) * (NR / 8) : ROT == 2 ? ((bh >> 3) & 3) * (NR / 4) : ROT == 3 ? ((bh >> 3) & 1) * (NR / 2) : ((bh >> 3) & 15) * (NR / 16);
  STAMP_AT(0);

  unsigned xv[4], ev[EI];
  {
    const int g = lane >> 4, c = lane & 15;
#pragma unroll
    for (int u = 0; u < 4; ++u) xv[u] = (unsigned)(w * XI * 1024 + g * 256 + ((c ^ ((g << 2) | ((w * XI + u) & 3))) << 4));
    constexpr int CPR = EL / 16, RPI = 64 / CPR;
#pragma unroll
    for (int u = 0; u < EI; ++u) {
      const int row = (w * EI + u) * RPI + lane / CPR, pos = lane % CPR;
      const int sw = EL == 256 ? (row & 15) : ((row >> 1) & 7);
      ev[u] = (unsigned)(row * (SKV * 2) + ((pos ^ sw) << 4) - u * 1024);
    }
  }
  auto issue = [&](int j, int slot) {
    const bool isv = j >= NR;
    const unsigned live = j < 2 * NR ? 1u : 0u;
    const u32x4 rx = srd(isv ? vp : kp, live * HEAD_BYTES), re = srd(isv ? F : E, live * (RR * SKV * 2));
    const int jj = (j + rot) & (NR - 1);
    const unsigned sx = jj * XB, se = jj * EL;
    const unsigned mx = lb + slot * SLOT + w * XI * 1024, me = lb + slot * SLOT + XB + w * EI * 1024;
    asm volatile("s_nop 4\n\ts_mov_b32 m0, %0\n\ts_nop 0\n\t"
                 "buffer_load_dwordx4 %1, %5, %6 offen nt lds\n\t"
                 "buffer_load_dwordx4 %2, %5, %6 offen offset:1024 nt lds\n\t"
                 "buffer_load_dwordx4 %3, %5, %6 offen offset:2048 nt lds\n\t"
                 "buffer_load_dwordx4 %4, %5, %6 offen offset:3072 nt lds"
                 ::"s"(mx), "v"(xv[0]), "v"(xv[1]), "v"(xv[2]), "v"(xv[3]), "s"(rx), "s"(sx) : "memory");
    if constexpr (XI == 8)
      asm volatile("s_mov_b32 m0, %0\n\ts_nop 0\n\t"
                   "buffer_load_dwordx4 %1, %5, %6 offen nt lds\n\t"
                   "buffer_load_dwordx4 %2, %5, %6 offen offset:1024 nt lds\n\t"
                   "buffer_load_dwordx4 %3, %5, %6 offen offset:2048 nt lds\n\t"
                   "buffer_load_dwordx4 %4, %5, %6 offen offset:3072 nt lds"
                   ::"s"(mx + 4096), "v"(xv[0]), "v"(xv[1]), "v"(xv[2]), "v"(xv[3]), "s"(rx), "s"(sx + 4096) : "memory");
    if constexpr (EI == 2)
      asm volatile("s_mov_b32 m0, %0\n\ts_nop 0\n\t"
                   "buffer_load_dwordx4 %1, %3, %4 offen " ENT " lds\n\t"
                   "buffer_load_dwordx4 %2, %3, %4 offen offset:1024 " ENT " lds"
                   ::"s"(me), "v"(ev[0]), "v"(ev[1]), "s"(re), "s"(se) : "memory");
    else
      asm volatile("s_mov_b32 m0, %0\n\ts_nop 0\n\t"
                   "buffer_load_dwordx4 %1, %5, %6 offen " ENT " lds\n\t"
                   "buffer_load_dwordx4 %2, %5, %6 offen offset:1024 " ENT " lds\n\t"
                   "buffer_load_dwordx4 %3, %5, %6 offen offset:2048 " ENT " lds\n\t"
                   "buffer_load_dwordx4 %4, %5, %6 offen offset:3072 " ENT " lds"
                   ::"s"(me), "v"(ev[0]), "v"(ev[1]), "v"(ev[EI - 2]), "v"(ev[EI - 1]), "s"(re), "s"(se) : "memory");
  };
  STAMP_AT(98);
  issue(0, 0);
  STAMP_AT(99);
  if (w == 0) {
    const u32x4 rq = srd(Q + (size_t)bh * (SQ * DH * 2), SQ * DH * 2);
    asm volatile("s_nop 4\n\ts_mov_b32 m0, %0\n\ts_nop 0\n\tbuffer_load_dwordx4 %1, %2, 0 offen lds" ::"s"(lb + Q_OFF), "v"(lane * 16), "s"(rq) : "memory");
  }
  if (PF) {
    const u32x4 rp = srd(w < 2 ? E : F, RR * SKV * 2);
    const unsigned so = (((bh >> 3) & 31) * 2 + (w & 1)) * 4096;
    asm volatile("s_nop 4\n\ts_mov_b32 m0, %0\n\ts_nop 0\n\t"
                 "buffer_load_dwordx4 %1, %2, %3 offen lds\n\t"
                 "buffer_load_dwordx4 %1, %2, %3 offen offset:1024 lds\n\t"
                 "buffer_load_dwordx4 %1, %2, %3 offen offset:2048 lds\n\t"
                 "buffer_load_dwordx4 %1, %2, %3 offen offset:3072 lds"
                 ::"s"(lb + PF_OFF), "v"(lane * 16), "s"(rp), "s"(so) : "memory");
  }
#pragma unroll
  for (int j = 1; j < DEPTH - 1; ++j) issue(j, j);

  const int h = lane >> 5, m = lane & 31;
  unsigned xr[2], er;
  {
    const int blk = (lane >> 4) & 1, q = (lane & 15) >> 2, p = lane & 3;
#pragma unroll
    for (int t = 0; t < 2; ++t) xr[t] = lb + 256 * (8 * h + 4 * t + q) + 16 * (((w ^ q) << 2) | ((2 * blk + (p >> 1)) ^ (2 * h + t))) + 8 * (p & 1);
    const int sw = EL == 256 ? (m & 15) : ((m >> 1) & 7);
    er = lb + XB + m * EL + 16 * (h ^ sw);
  }
  f32x16 acc[2];
  int slot = 0;
  auto round = [&](int i, bool isK) {
    wait_vm<(DEPTH - 2) * NI>();
    __builtin_amdgcn_s_barrier();
    asm volatile("" ::: "memory");
    STAMP_AT(1 + i);
    issue(i + DEPTH - 1, slot == 0 ? DEPTH - 1 : slot - 1);
    const unsigned so = slot * SLOT;
    const unsigned xa0 = xr[0] + so, xa1 = xr[1] + so, ea = er + so;
    u32x4 xf[KS], e0[KS], e1[KS];
#pragma unroll
    for (int ks = 0; ks < KS; ++ks) {
      const u32x2 x0 = trd(xa0 + 4096 * ks), x1 = trd(xa1 + 4096 * ks);
      xf[ks] = u32x4{x0[0], x0[1], x1[0], x1[1]};
      e0[ks] = *(const LAS u32x4*)(ea ^ (32 * ks));
      e1[ks] = *(const LAS u32x4*)((ea ^ (32 * ks)) + 32 * EL);
    }
    __builtin_amdgcn_sched_barrier(0);
#pragma unroll
    for (int ks = 0; ks < KS; ++ks) {
      acc[0] = isK ? mfma(xf[ks], e0[ks], acc[0]) : mfma(e0[ks], xf[ks], acc[0]);
      acc[1] = isK ? mfma(xf[ks], e1[ks], acc[1]) : mfma(e1[ks], xf[ks], acc[1]);
    }
    __builtin_amdgcn_sched_barrier(0);
    asm volatile("s_waitcnt lgkmcnt(0)" ::: "memory");
    slot = slot == DEPTH - 1 ? 0 : slot + 1;
  };
  auto cvt8 = [&](const f32x16& x, int s) {
    return u32x4{pk(x[8 * s], x[8 * s + 1]), pk(x[8 * s + 2], x[8 * s + 3]), pk(x[8 * s + 4], x[8 * s + 5]), pk(x[8 * s + 6], x[8 * s + 7])};
  };
  const f32x16 zero16 = {0.f, 0.f, 0.f, 0.f, 0.f, 0.f, 0.f, 0.f, 0.f, 0.f, 0.f, 0.f, 0.f, 0.f, 0.f, 0.f};

  acc[0] = zero16; acc[1] = zero16;
#pragma unroll 1
  for (int i = 0; i < NR; ++i) round(i, true);

  {
    LAS float* sc = (LAS float*)(lb + SC_OFF);
    u32x4 qf[2];
#pragma unroll
    for (int s = 0; s < 2; ++s) {
      const unsigned qa = lb + Q_OFF + (m & 3) * 256 + (32 * w + 16 * s + 4 * h) * 2;
      const u32x2 a = *(const LAS u32x2*)qa, b = *(const LAS u32x2*)(qa + 16);
      qf[s] = u32x4{a[0], a[1], b[0], b[1]};
    }
#pragma unroll
    for (int rb = 0; rb < 2; ++rb) {
      f32x16 s = mfma(qf[0], cvt8(acc[rb], 0), zero16);
      s = mfma(qf[1], cvt8(acc[rb], 1), s);
      if (h == 0) {
#pragma unroll
        for (int qi = 0; qi < 4; ++qi) sc[(w * SQ + qi) * RR + 32 * rb + m] = s[qi];
      }
    }
    asm volatile("s_waitcnt lgkmcnt(0)" ::: "memory");
  }
  acc[0] = zero16; acc[1] = zero16;
  round(NR, false);
  u32x4 pf[4];
  {
    const LAS float* sc = (const LAS float*)(lb + SC_OFF) + (m & 3) * RR + 4 * h;
    f32x4 sv[8];
#pragma unroll
    for (int u = 0; u < 8; ++u) {
      f32x4 a = *(const LAS f32x4*)(sc + 8 * u);
#pragma unroll
      for (int ww = 1; ww < NW; ++ww) a += *(const LAS f32x4*)(sc + ww * SQ * RR + 8 * u);
      sv[u] = a * (0.08838834764831845f * 1.4426950408889634f);
    }
    float mx = -INFINITY;
#pragma unroll
    for (int u = 0; u < 8; ++u)
#pragma unroll
      for (int i = 0; i < 4; ++i) mx = fmaxf(mx, sv[u][i]);
    mx = fmaxf(mx, __shfl_xor(mx, 32));
    float l = 0.f;
#pragma unroll
    for (int u = 0; u < 8; ++u)
#pragma unroll
      for (int i = 0; i < 4; ++i) { sv[u][i] = __builtin_amdgcn_exp2f(sv[u][i] - mx); l += sv[u][i]; }
    l += __shfl_xor(l, 32);
    const float inv = 1.0f / l;
#pragma unroll
    for (int s = 0; s < 4; ++s)
      pf[s] = u32x4{pk(sv[2 * s][0] * inv, sv[2 * s][1] * inv), pk(sv[2 * s][2] * inv, sv[2 * s][3] * inv),
                    pk(sv[2 * s + 1][0] * inv, sv[2 * s + 1][1] * inv), pk(sv[2 * s + 1][2] * inv, sv[2 * s + 1][3] * inv)};
  }
#pragma unroll 1
  for (int i = NR + 1; i < 2 * NR; ++i) round(i, false);

  {
    f32x16 o0 = mfma(cvt8(acc[0], 0), pf[0], zero16);
    f32x16 o1 = mfma(cvt8(acc[1], 0), pf[2], zero16);
    o0 = mfma(cvt8(acc[0], 1), pf[1], o0);
    o1 = mfma(cvt8(acc[1], 1), pf[3], o1);
    if (m < 4) {
      char* op = (char*)O + (size_t)bh * (SQ * DH * 2) + m * (DH * 2) + (32 * w + 4 * h) * 2;
#pragma unroll
      for (int gq = 0; gq < 4; ++gq)
        *(u32x2*)(op + 16 * gq) = u32x2{pk(o0[4 * gq] + o1[4 * gq], o0[4 * gq + 1] + o1[4 * gq + 1]), pk(o0[4 * gq + 2] + o1[4 * gq + 2], o0[4 * gq + 3] + o1[4 * gq + 3])};
    }
  }
  if (bh == 7) while ((long long)(__builtin_amdgcn_s_memrealtime() - tt0) < 6000) __builtin_amdgcn_s_sleep(1);
}

extern "C" void kernel_launch(void* const* d_in, const int* in_sizes, int n_in, void* d_out, int out_size, void* d_ws, size_t ws_size,
                              hipStream_t stream) {
  linformer_kernel<<<256, 256, 0, stream>>>((const char*)d_in[0], (const char*)d_in[1], (const char*)d_in[2], (const char*)d_in[3],
                                           (const char*)d_in[4], (__hip_bfloat16*)d_out
  );
}
```

```cpp
#include <hip/hip_runtime.h>
#include <hip/hip_bf16.h>
#include <type_traits>

typedef unsigned u32x4 __attribute__((ext_vector_type(4)));
typedef unsigned u32x2 __attribute__((ext_vector_type(2)));
typedef unsigned u32x8 __attribute__((ext_vector_type(8)));
typedef float f32x4 __attribute__((ext_vector_type(4)));
typedef float f32x16 __attribute__((ext_vector_type(16)));
typedef short s16x4 __attribute__((ext_vector_type(4)));
typedef __bf16 bf16x8 __attribute__((ext_vector_type(8)));
typedef __bf16 bf16x2 __attribute__((ext_vector_type(2)));
#define DI __device__ __forceinline__
#define LAS __attribute__((address_space(3)))

#define DEPTH 4
#define TW 3
#define FTR (NR + 4)
#define PF 5
#define STPOL ""
constexpr int SKV = 2048, DH = 128, RR = 64, SQ = 4, NW = 4;
constexpr int RN = 64;
constexpr int HEAD_BYTES = SKV * DH * 2;
constexpr int NR = SKV / RN;
constexpr int XB = RN * 256;
constexpr int EL = RN * 2;
constexpr int EB = RR * EL;
constexpr int SLOT = XB + EB;
constexpr int NI = 12;
constexpr int KS = RN / 16;
constexpr int FT_OFF = DEPTH * SLOT;
constexpr int Q_OFF = FT_OFF + TW * EB, SC_OFF = Q_OFF + 1024, P_OFF = SC_OFF + NW * SQ * RR * 4, ST_OFF = P_OFF + 512, PF_OFF = ST_OFF + 1024,
              LDS_USED = PF_OFF + 512, LDS_BYTES = LDS_USED > 83968 ? LDS_USED : 83968;
static_assert(LDS_BYTES <= 163840, "LDS");
static_assert((DEPTH - 1) * NI + 4 * TW + 2 <= 63 && TW >= 1 && TW <= DEPTH - 1, "vmcnt / tail");

DI f32x16 mfma(u32x4 a, u32x4 b, f32x16 c) {
  return __builtin_amdgcn_mfma_f32_32x32x16_bf16(__builtin_bit_cast(bf16x8, a), __builtin_bit_cast(bf16x8, b), c, 0, 0, 0);
}
DI unsigned pk(float lo, float hi) {
  bf16x2 v = {(__bf16)lo, (__bf16)hi};
  return __builtin_bit_cast(unsigned, v);
}
template <typename T> DI LAS T* lp(unsigned a) { return (LAS T*)(size_t)a; }
DI u32x2 trd(unsigned a) { return __builtin_bit_cast(u32x2, __builtin_amdgcn_ds_read_tr16_b64_v4i16(lp<s16x4>(a))); }
DI u32x4 trd2(unsigned a0, unsigned a1) { const u32x2 x = trd(a0), y = trd(a1); return u32x4{x[0], x[1], y[0], y[1]}; }
DI u32x4 srd(const char* p, unsigned bytes) {
  const unsigned long long a = (unsigned long long)p;
  return u32x4{(unsigned)a, (unsigned)(a >> 32) & 0xffffu, bytes, 0x00020000u};
}
template <int N> DI void wait_vm() { asm volatile("s_waitcnt vmcnt(%0)" ::"n"(N) : "memory"); }
template <int CTRL> DI float dpp(float x) { return __builtin_bit_cast(float, __builtin_amdgcn_mov_dpp(__builtin_bit_cast(int, x), CTRL, 0xf, 0xf, true)); }
DI float wave_max(float x) {
  x = fmaxf(x, dpp<0x128>(x)); x = fmaxf(x, dpp<0x124>(x)); x = fmaxf(x, dpp<0x122>(x)); x = fmaxf(x, dpp<0x121>(x));
  auto s = __builtin_amdgcn_permlane16_swap(__float_as_uint(x), __float_as_uint(x), false, false);
  x = fmaxf(__uint_as_float(s[0]), __uint_as_float(s[1]));
  auto t = __builtin_amdgcn_permlane32_swap(__float_as_uint(x), __float_as_uint(x), false, false);
  return fmaxf(__uint_as_float(t[0]), __uint_as_float(t[1]));
}
DI float wave_sum(float x) {
  x += dpp<0x128>(x); x += dpp<0x124>(x); x += dpp<0x122>(x); x += dpp<0x121>(x);
  auto s = __builtin_amdgcn_permlane16_swap(__float_as_uint(x), __float_as_uint(x), false, false);
  x = __uint_as_float(s[0]) + __uint_as_float(s[1]);
  auto t = __builtin_amdgcn_permlane32_swap(__float_as_uint(x), __float_as_uint(x), false, false);
  return __uint_as_float(t[0]) + __uint_as_float(t[1]);
}
DI u32x4 cvt8(const f32x16& x, int s) {
  return u32x4{pk(x[8 * s], x[8 * s + 1]), pk(x[8 * s + 2], x[8 * s + 3]), pk(x[8 * s + 4], x[8 * s + 5]), pk(x[8 * s + 6], x[8 * s + 7])};
}
template <int V> using IC = std::integral_constant<int, V>;

#define STAMP_ARG
#define STAMP_AT(i)
__global__ __launch_bounds__(512) void linformer_kernel(const char* __restrict__ Q_, const char* __restrict__ K_, const char* __restrict__ V_,
                                                         const char* __restrict__ E_, const char* __restrict__ F_, char* __restrict__ O_ STAMP_ARG) {
  __shared__ __attribute__((aligned(1024))) char lds[LDS_BYTES];
  u32x8 ka0; u32x4 ka1;
  asm volatile("s_load_dwordx8 %0, %2, 0x0\n\ts_load_dwordx4 %1, %2, 0x20" : "=&s"(ka0), "=&s"(ka1) : "s"(__builtin_amdgcn_kernarg_segment_ptr()));
  const int tid = threadIdx.x, lane = tid & 63, l = __builtin_amdgcn_readfirstlane(tid >> 6), w = l - 2;
  const int bid = blockIdx.x;
  const int bh = (bid & 7) * 4 + ((bid >> 3) & 3) + (bid >> 5) * 32;
  const unsigned lb = (unsigned)(size_t)(LAS char*)lds;
  const int rot = (bid >> 3) & 31;
  STAMP_AT(0);

  constexpr int IEND = 2 * NR - TW - DEPTH + 1;
      unsigned xv[4], ev[2];
      {
        const int g = lane >> 4, c = lane & 15;
  #pragma unroll
        for (int u = 0; u < 4; ++u) xv[u] = (unsigned)(l * 8192 + g * 256 + ((c ^ ((g << 2) | u)) << 4));
  #pragma unroll
        for (int u = 0; u < 2; ++u) {
          const int row = l * 32 + u * 8 + (lane >> 3), pos = lane & 7;
          ev[u] = (unsigned)(row * (SKV * 2) + ((pos ^ ((row >> 1) & 7)) << 4) - u * 1024);
        }
      }
      asm volatile("" ::"v"(xv[0]), "v"(xv[1]), "v"(xv[2]), "v"(xv[3]), "v"(ev[0]), "v"(ev[1]));
  asm volatile("s_waitcnt lgkmcnt(0)" : "+s"(ka0), "+s"(ka1));
  auto mkp = [](unsigned lo, unsigned hi) { return (char*)(((unsigned long long)hi << 32) | lo); };
  const char *Q = mkp(ka0[0], ka0[1]), *K = mkp(ka0[2], ka0[3]), *V = mkp(ka0[4], ka0[5]), *E = mkp(ka0[6], ka0[7]), *F = mkp(ka1[0], ka1[1]);
  char* O = mkp(ka1[2], ka1[3]);
  const char* kp = K + (size_t)bh * HEAD_BYTES;
  const char* vp = V + (size_t)bh * HEAD_BYTES;
  auto issue_x = [&](int j, int slot) {
        const u32x4 rx = srd(j >= NR ? vp : kp, HEAD_BYTES);
        const unsigned sx = ((j + rot) & (NR - 1)) * XB, mx = lb + slot * SLOT + l * 8192;
        asm volatile("s_nop 4\n\ts_mov_b32 m0, %0\n\ts_nop 0\n\t"
                     "buffer_load_dwordx4 %1, %5, %6 offen nt lds\n\t"
                     "buffer_load_dwordx4 %2, %5, %6 offen offset:1024 nt lds\n\t"
                     "buffer_load_dwordx4 %3, %5, %6 offen offset:2048 nt lds\n\t"
                     "buffer_load_dwordx4 %4, %5, %6 offen offset:3072 nt lds\n\t"
                     "s_mov_b32 m0, %7\n\ts_nop 0\n\t"
                     "buffer_load_dwordx4 %1, %5, %8 offen nt lds\n\t"
                     "buffer_load_dwordx4 %2, %5, %8 offen offset:1024 nt lds\n\t"
                     "buffer_load_dwordx4 %3, %5, %8 offen offset:2048 nt lds\n\t"
                     "buffer_load_dwordx4 %4, %5, %8 offen offset:3072 nt lds"
                     ::"s"(mx), "v"(xv[0]), "v"(xv[1]), "v"(xv[2]), "v"(xv[3]), "s"(rx), "s"(sx), "s"(mx + 4096), "s"(sx + 4096) : "memory");
      };
      auto issue_e = [&](int j, unsigned dst) {
        const u32x4 re = srd(j >= NR ? F : E, RR * SKV * 2);
        const unsigned se = ((j + rot) & (NR - 1)) * EL, me = lb + dst + l * 4096;
        asm volatile("s_nop 4\n\ts_mov_b32 m0, %0\n\ts_nop 0\n\t"
                     "buffer_load_dwordx4 %1, %3, %4 offen lds\n\t"
                     "buffer_load_dwordx4 %2, %3, %4 offen offset:1024 lds\n\t"
                     "s_mov_b32 m0, %5\n\ts_nop 0\n\t"
                     "buffer_load_dwordx4 %1, %3, %6 offen lds\n\t"
                     "buffer_load_dwordx4 %2, %3, %6 offen offset:1024 lds"
                     ::"s"(me), "v"(ev[0]), "v"(ev[1]), "s"(re), "s"(se), "s"(me + 2048), "s"(se + 16 * SKV * 2) : "memory");
      };
  if (l < 2) {
    if (PF == 5) {
      const u32x4 rp = srd(l == 0 ? E : F, RR * SKV * 2);
      asm volatile("s_nop 4\n\ts_mov_b32 m0, %0\n\ts_nop 0\n\tbuffer_load_dword %1, %2, %3 offen lds"
                   ::"s"(lb + PF_OFF + l * 256), "v"(lane * 128), "s"(rp), "s"(rot * 8192) : "memory");
    }
    issue_x(0, 0); issue_e(0, XB);
    if (l == 1) {
      const u32x4 rq = srd(Q + (size_t)bh * (SQ * DH * 2), SQ * DH * 2);
      asm volatile("s_nop 4\n\ts_mov_b32 m0, %0\n\ts_nop 0\n\tbuffer_load_dwordx4 %1, %2, 0 offen lds" ::"s"(lb + Q_OFF), "v"(lane * 16), "s"(rq) : "memory");
    }
#pragma unroll
    for (int j = 1; j < DEPTH - 1; ++j) { issue_x(j, j); issue_e(j, j * SLOT + XB); }
  }
  if (__builtin_amdgcn_readfirstlane(tid) < 128) {
    constexpr int WF = (DEPTH - 2) * NI;
    int ps = DEPTH - 1;
#pragma unroll 1
    for (int i = 0; i < IEND; ++i) {
      if (i > FTR && i < FTR + DEPTH - 1) wait_vm<WF + 4 * TW>(); else wait_vm<WF>();
      __builtin_amdgcn_s_barrier();
      issue_x(i + DEPTH - 1, ps); issue_e(i + DEPTH - 1, ps * SLOT + XB);
      if (i == FTR) {
#pragma unroll
        for (int u = 0; u < TW; ++u) issue_e(2 * NR - TW + u, FT_OFF + u * EB);
      }
      ps = ps == DEPTH - 1 ? 0 : ps + 1;
    }
    auto lround = [&](auto ic) {
      constexpr int i = decltype(ic)::value;
      constexpr int f0 = 2 * NR - TW - 1 - i, nfull = f0 < 0 ? 0 : f0 < DEPTH - 2 ? f0 : DEPTH - 2;
      constexpr int last = (i + DEPTH - 2) < (2 * NR - 1) ? (i + DEPTH - 2) : (2 * NR - 1);
      constexpr int first = (i + 1) > (2 * NR - TW) ? (i + 1) : (2 * NR - TW);
      constexpr int nx = last - first + 1 > 0 ? last - first + 1 : 0;
      wait_vm<nfull * NI + nx * 8>();
      __builtin_amdgcn_s_barrier();
      if constexpr (i + DEPTH - 1 < 2 * NR) issue_x(i + DEPTH - 1, (i + DEPTH - 1) % DEPTH);
    };
    lround(IC<IEND>{});
    if constexpr (IEND + 1 < 2 * NR) lround(IC<IEND + 1>{});
    if constexpr (IEND + 2 < 2 * NR) lround(IC<IEND + 2>{});
    if constexpr (IEND + 3 < 2 * NR) lround(IC<IEND + 3>{});
    if constexpr (IEND + 4 < 2 * NR) lround(IC<IEND + 4>{});
    if constexpr (IEND + 5 < 2 * NR) lround(IC<IEND + 5>{});
    if constexpr (IEND + 6 < 2 * NR) lround(IC<IEND + 6>{});
    if constexpr (IEND + 7 < 2 * NR) lround(IC<IEND + 7>{});
    static_assert(IEND + 8 >= 2 * NR, "tail rounds");
    return;
  }

  const int h = lane >> 5, m = lane & 31;
  const int blk = (lane >> 4) & 1, q4 = (lane & 15) >> 2, p4 = lane & 3;
  unsigned xr[2], er;
#pragma unroll
  for (int t = 0; t < 2; ++t) xr[t] = lb + 256 * (8 * h + 4 * t + q4) + 16 * (((w ^ q4) << 2) | ((2 * blk + (p4 >> 1)) ^ (2 * h + t))) + 8 * (p4 & 1);
  er = lb + XB + m * EL + 16 * (h ^ ((m >> 1) & 7));
  f32x16 acc[2];
  int slot = 0;
  auto round = [&](int i, bool isK) {
    __builtin_amdgcn_s_barrier();
    asm volatile("" ::: "memory");
    STAMP_AT(1 + i);
    const unsigned so = slot * SLOT;
    const unsigned xa0 = xr[0] + so, xa1 = xr[1] + so, ea = er + so;
    u32x4 xf[KS], e0[KS], e1[KS];
#pragma unroll
    for (int ks = 0; ks < KS; ++ks) {
      xf[ks] = trd2(xa0 + 4096 * ks, xa1 + 4096 * ks);
      e0[ks] = *lp<const u32x4>(ea ^ (32 * ks));
      e1[ks] = *lp<const u32x4>((ea ^ (32 * ks)) + 32 * EL);
      __builtin_amdgcn_sched_barrier(0);
    }
#pragma unroll
    for (int ks = 0; ks < KS; ++ks) {
      acc[0] = isK ? mfma(xf[ks], e0[ks], acc[0]) : mfma(e0[ks], xf[ks], acc[0]);
      acc[1] = isK ? mfma(xf[ks], e1[ks], acc[1]) : mfma(e1[ks], xf[ks], acc[1]);
    }
    __builtin_amdgcn_sched_barrier(0);
    asm volatile("s_waitcnt lgkmcnt(0)" ::: "memory");
    slot = slot == DEPTH - 1 ? 0 : slot + 1;
  };
  const f32x16 zero16 = {0.f, 0.f, 0.f, 0.f, 0.f, 0.f, 0.f, 0.f, 0.f, 0.f, 0.f, 0.f, 0.f, 0.f, 0.f, 0.f};

  acc[0] = zero16; acc[1] = zero16;
#pragma unroll 1
  for (int i = 0; i < NR; ++i) round(i, true);

  {
    LAS float* sc = lp<float>(lb + SC_OFF);
    u32x4 qf[2];
#pragma unroll
    for (int s = 0; s < 2; ++s) {
      const unsigned qa = lb + Q_OFF + (m & 3) * 256 + (32 * w + 16 * s + 4 * h) * 2;
      const u32x2 a = *lp<const u32x2>(qa), b = *lp<const u32x2>(qa + 16);
      qf[s] = u32x4{a[0], a[1], b[0], b[1]};
    }
    f32x16 s0 = mfma(qf[0], cvt8(acc[0], 0), zero16), s1 = mfma(qf[0], cvt8(acc[1], 0), zero16);
    s0 = mfma(qf[1], cvt8(acc[0], 1), s0); s1 = mfma(qf[1], cvt8(acc[1], 1), s1);
    if (h == 0) {
#pragma unroll
      for (int qi = 0; qi < 4; ++qi) { sc[(w * SQ + qi) * RR + m] = s0[qi]; sc[(w * SQ + qi) * RR + 32 + m] = s1[qi]; }
    }
    asm volatile("s_waitcnt lgkmcnt(0)" ::: "memory");
  }
  acc[0] = zero16; acc[1] = zero16;
  round(NR, false);
  {
    const LAS float* sc = lp<const float>(lb + SC_OFF) + w * RR + lane;
    const float s = ((sc[0] + sc[SQ * RR]) + (sc[2 * SQ * RR] + sc[3 * SQ * RR])) * (0.08838834764831845f * 1.4426950408889634f);
    const float e = __builtin_amdgcn_exp2f(s - wave_max(s));
    const float p = e * __builtin_amdgcn_rcpf(wave_sum(e));
    *lp<unsigned short>(lb + P_OFF + (w * RR + lane) * 2) = (unsigned short)pk(p, p);
    asm volatile("s_waitcnt lgkmcnt(0)" ::: "memory");
  }
  u32x4 wf[TW][KS];
  auto wt = [&](int u, int c) {
    f32x16 d = zero16;
    unsigned fa[2];
#pragma unroll
    for (int t = 0; t < 2; ++t)
      fa[t] = lb + FT_OFF + u * EB + EL * (8 * h + 4 * t + q4) + 16 * ((4 * c + 2 * blk + (p4 >> 1)) ^ (4 * h + 2 * t + (q4 >> 1))) + 8 * (p4 & 1);
#pragma unroll
    for (int ks = 0; ks < 4; ++ks)
      d = mfma(trd2(fa[0] + 16 * EL * ks, fa[1] + 16 * EL * ks), *lp<const u32x4>(lb + P_OFF + (m & 3) * (RR * 2) + (16 * ks + 8 * h) * 2), d);
    wf[u][2 * c] = cvt8(d, 0); wf[u][2 * c + 1] = cvt8(d, 1);
    asm volatile("s_waitcnt lgkmcnt(0)" ::: "memory");
  };
#pragma unroll 1
  for (int i = NR + 1; i < 2 * NR - TW; ++i) {
    round(i, false);
#pragma unroll
    for (int u = 0; u < TW; ++u) {
      if (i == FTR + DEPTH + 1 + 2 * u) wt(u, 0);
      if (i == FTR + DEPTH + 2 + 2 * u) wt(u, 1);
    }
  }
  u32x4 pf[4];
#pragma unroll
  for (int s = 0; s < 4; ++s) {
    const unsigned pa = lb + P_OFF + (m & 3) * (RR * 2) + (16 * s + 4 * h) * 2;
    const u32x2 a = *lp<const u32x2>(pa), b = *lp<const u32x2>(pa + 16);
    pf[s] = u32x4{a[0], a[1], b[0], b[1]};
  }
  f32x16 o = mfma(cvt8(acc[0], 0), pf[0], zero16);
  o = mfma(cvt8(acc[1], 0), pf[2], o);
  o = mfma(cvt8(acc[0], 1), pf[1], o);
  o = mfma(cvt8(acc[1], 1), pf[3], o);
  unsigned xt[2];
#pragma unroll
  for (int t = 0; t < 2; ++t) xt[t] = lb + 256 * (8 * t + 4 * h + q4) + 16 * (((w ^ q4) << 2) | ((2 * blk + (p4 >> 1)) ^ (2 * t + h))) + 8 * (p4 & 1);
  char* op = O + (size_t)bh * (SQ * DH * 2) + (m & 3) * (DH * 2) + (32 * w + 4 * h) * 2;
  auto tail = [&](auto uc) {
    constexpr int u = decltype(uc)::value, i = 2 * NR - TW + u;
    __builtin_amdgcn_s_barrier();
    asm volatile("" ::: "memory");
    STAMP_AT(1 + i);
    constexpr unsigned so = (i % DEPTH) * SLOT;
    u32x4 xf[KS];
#pragma unroll
    for (int ks = 0; ks < KS; ++ks) { xf[ks] = trd2(xt[0] + so + 4096 * ks, xt[1] + so + 4096 * ks); __builtin_amdgcn_sched_barrier(0); }
#pragma unroll
    for (int ks = 0; ks < KS; ++ks) o = mfma(xf[ks], wf[u][ks], o);
    __builtin_amdgcn_sched_barrier(0);
  };
  tail(IC<0>{});
  if constexpr (TW >= 2) tail(IC<1>{});
  if constexpr (TW >= 3) tail(IC<2>{});
  if constexpr (TW >= 4) tail(IC<3>{});
  if (m < 4) {
#pragma unroll
    for (int gq = 0; gq < 4; ++gq) {
      const u32x2 val = u32x2{pk(o[4 * gq], o[4 * gq + 1]), pk(o[4 * gq + 2], o[4 * gq + 3])};
      asm volatile("global_store_dwordx2 %0, %1, off offset:%2 " STPOL ::"v"(op), "v"(val), "n"(16 * gq) : "memory");
    }
  }
}

extern "C" void kernel_launch(void* const* d_in, const int* in_sizes, int n_in, void* d_out, int out_size, void* d_ws, size_t ws_size,
                              hipStream_t stream) {
  linformer_kernel<<<256, 384, 0, stream>>>((const char*)d_in[0], (const char*)d_in[1], (const char*)d_in[2], (const char*)d_in[3],
                                           (const char*)d_in[4], (char*)d_out
  );
}
```

```cpp
#include <hip/hip_runtime.h>
#include <hip/hip_bf16.h>

typedef unsigned u32x2 __attribute__((ext_vector_type(2)));
typedef unsigned u32x4 __attribute__((ext_vector_type(4)));
typedef unsigned u32x8 __attribute__((ext_vector_type(8)));
typedef float f32x16 __attribute__((ext_vector_type(16)));
typedef short s16x4 __attribute__((ext_vector_type(4)));
typedef __bf16 bf16x8 __attribute__((ext_vector_type(8)));
typedef __bf16 bf16x2 __attribute__((ext_vector_type(2)));
#define DI __device__ __forceinline__
#define LAS __attribute__((address_space(3)))

constexpr int SKV = 2048, DH = 128, RR = 64, SQ = 4, NW = 4;
constexpr int HEAD_BYTES = SKV * DH * 2, PROJ_BYTES = RR * SKV * 2;
constexpr int RN = 64;
constexpr int NR = SKV / RN;
constexpr int XB = RN * 256;
constexpr int EL = RN * 2;
constexpr int EB = RR * EL;
constexpr int SLOT = XB + EB, DEPTH = 4;
constexpr int NI = 12;
constexpr int KS = RN / 16;
constexpr int TW = 3;
constexpr int FTR = NR + 4;
constexpr int FT_OFF = DEPTH * SLOT;
constexpr int Q_OFF = FT_OFF + TW * EB, SC_OFF = Q_OFF + 1024, P_OFF = SC_OFF + NW * SQ * RR * 4, PF_OFF = P_OFF + 512, LDS_BYTES = PF_OFF + 512;
static_assert(LDS_BYTES <= 163840 && LDS_BYTES > 81920, "one workgroup per CU");
static_assert((DEPTH - 1) * NI + 4 * TW + 2 <= 63 && TW * EB >= XB, "vmcnt range / reuse of the F area");

DI f32x16 mfma(u32x4 a, u32x4 b, f32x16 c) {
  return __builtin_amdgcn_mfma_f32_32x32x16_bf16(__builtin_bit_cast(bf16x8, a), __builtin_bit_cast(bf16x8, b), c, 0, 0, 0);
}
DI unsigned pk(float lo, float hi) {
  bf16x2 v = {(__bf16)lo, (__bf16)hi};
  return __builtin_bit_cast(unsigned, v);
}
DI u32x4 cvt8(const f32x16& x, int s) {
  return u32x4{pk(x[8 * s], x[8 * s + 1]), pk(x[8 * s + 2], x[8 * s + 3]), pk(x[8 * s + 4], x[8 * s + 5]), pk(x[8 * s + 6], x[8 * s + 7])};
}
template <typename T> DI LAS T* lp(unsigned a) { return (LAS T*)(size_t)a; }
DI u32x4 trd2(unsigned a0, unsigned a1) {
  const u32x2 x = __builtin_bit_cast(u32x2, __builtin_amdgcn_ds_read_tr16_b64_v4i16(lp<s16x4>(a0)));
  const u32x2 y = __builtin_bit_cast(u32x2, __builtin_amdgcn_ds_read_tr16_b64_v4i16(lp<s16x4>(a1)));
  return u32x4{x[0], x[1], y[0], y[1]};
}
DI u32x4 rd2(unsigned a) {
  const u32x2 x = *lp<const u32x2>(a), y = *lp<const u32x2>(a + 16);
  return u32x4{x[0], x[1], y[0], y[1]};
}
DI u32x4 srd(const char* p, unsigned bytes) {
  const unsigned long long a = (unsigned long long)p;
  return u32x4{(unsigned)a, (unsigned)(a >> 32) & 0xffffu, bytes, 0x00020000u};
}
template <int N> DI void wait_vm() { asm volatile("s_waitcnt vmcnt(%0)" ::"n"(N) : "memory"); }
DI void wait_lds() { asm volatile("s_waitcnt lgkmcnt(0)" ::: "memory"); }
DI void barrier() { __builtin_amdgcn_s_barrier(); asm volatile("" ::: "memory"); }
template <int CTRL> DI float dpp(float x) { return __builtin_bit_cast(float, __builtin_amdgcn_mov_dpp(__builtin_bit_cast(int, x), CTRL, 0xf, 0xf, true)); }
DI float wave_max(float x) {
  x = fmaxf(x, dpp<0x128>(x)); x = fmaxf(x, dpp<0x124>(x)); x = fmaxf(x, dpp<0x122>(x)); x = fmaxf(x, dpp<0x121>(x));
  const auto s = __builtin_amdgcn_permlane16_swap(__float_as_uint(x), __float_as_uint(x), false, false);
  x = fmaxf(__uint_as_float(s[0]), __uint_as_float(s[1]));
  const auto t = __builtin_amdgcn_permlane32_swap(__float_as_uint(x), __float_as_uint(x), false, false);
  return fmaxf(__uint_as_float(t[0]), __uint_as_float(t[1]));
}
DI float wave_sum(float x) {
  x += dpp<0x128>(x); x += dpp<0x124>(x); x += dpp<0x122>(x); x += dpp<0x121>(x);
  const auto s = __builtin_amdgcn_permlane16_swap(__float_as_uint(x), __float_as_uint(x), false, false);
  x = __uint_as_float(s[0]) + __uint_as_float(s[1]);
  const auto t = __builtin_amdgcn_permlane32_swap(__float_as_uint(x), __float_as_uint(x), false, false);
  return __uint_as_float(t[0]) + __uint_as_float(t[1]);
}

__global__ __launch_bounds__(512) void linformer_kernel(const char* Q_, const char* K_, const char* V_, const char* E_, const char* F_, char* O_) {
  __shared__ __attribute__((aligned(1024))) char lds[LDS_BYTES];
  u32x8 ka0; u32x4 ka1;
  const auto kap = __builtin_amdgcn_kernarg_segment_ptr();
  asm volatile("s_load_dwordx8 %0, %2, 0x0\n\ts_load_dwordx4 %1, %2, 0x20" : "=&s"(ka0), "=&s"(ka1) : "s"(kap));
  __builtin_amdgcn_sched_barrier(0);
  const int tid = threadIdx.x, lane = tid & 63, l = __builtin_amdgcn_readfirstlane(tid >> 6), w = l - 2;
  const int bid = blockIdx.x;
  const int bh = (bid & 7) * 4 + ((bid >> 3) & 3) + (bid >> 5) * 32;
  const int rot = (bid >> 3) & 31;
  const unsigned lb = (unsigned)(size_t)(LAS char*)lds;

  unsigned xv[4], ev[2];
#pragma unroll
  for (int u = 0; u < 4; ++u) xv[u] = (unsigned)(l * 8192 + (lane >> 4) * 256 + (((lane & 15) ^ (((lane >> 4) << 2) | u)) << 4));
#pragma unroll
  for (int u = 0; u < 2; ++u) {
    const int row = l * 32 + u * 8 + (lane >> 3);
    ev[u] = (unsigned)(row * (SKV * 2) + (((lane & 7) ^ ((row >> 1) & 7)) << 4) - u * 1024);
  }
  asm volatile("" ::"v"(xv[0]), "v"(xv[1]), "v"(xv[2]), "v"(xv[3]), "v"(ev[0]), "v"(ev[1]));
  asm volatile("s_waitcnt lgkmcnt(0)" : "+s"(ka0), "+s"(ka1) : "s"(kap));
  auto mkp = [](unsigned lo, unsigned hi) { return (char*)(((unsigned long long)hi << 32) | lo); };
  const char *Q = mkp(ka0[0], ka0[1]), *K = mkp(ka0[2], ka0[3]), *V = mkp(ka0[4], ka0[5]), *E = mkp(ka0[6], ka0[7]), *F = mkp(ka1[0], ka1[1]);
  char* O = mkp(ka1[2], ka1[3]);
  const char* kp = K + (size_t)bh * HEAD_BYTES;
  const char* vp = V + (size_t)bh * HEAD_BYTES;

  auto issue_x = [&](int j, unsigned dst) {
    const u32x4 rx = srd(j >= NR ? vp : kp, HEAD_BYTES);
    const unsigned sx = ((j + rot) & (NR - 1)) * XB, mx = lb + dst + l * 8192;
    asm volatile("s_nop 4\n\ts_mov_b32 m0, %0\n\ts_nop 0\n\t"
                 "buffer_load_dwordx4 %1, %5, %6 offen nt lds\n\t"
                 "buffer_load_dwordx4 %2, %5, %6 offen offset:1024 nt lds\n\t"
                 "buffer_load_dwordx4 %3, %5, %6 offen offset:2048 nt lds\n\t"
                 "buffer_load_dwordx4 %4, %5, %6 offen offset:3072 nt lds\n\t"
                 "s_mov_b32 m0, %7\n\ts_nop 0\n\t"
                 "buffer_load_dwordx4 %1, %5, %8 offen nt lds\n\t"
                 "buffer_load_dwordx4 %2, %5, %8 offen offset:1024 nt lds\n\t"
                 "buffer_load_dwordx4 %3, %5, %8 offen offset:2048 nt lds\n\t"
                 "buffer_load_dwordx4 %4, %5, %8 offen offset:3072 nt lds"
                 ::"s"(mx), "v"(xv[0]), "v"(xv[1]), "v"(xv[2]), "v"(xv[3]), "s"(rx), "s"(sx), "s"(mx + 4096), "s"(sx + 4096) : "memory");
  };
  auto issue_e = [&](int j, unsigned dst) {
    const u32x4 re = srd(j >= NR ? F : E, PROJ_BYTES);
    const unsigned se = ((j + rot) & (NR - 1)) * EL, me = lb + dst + l * 4096;
    asm volatile("s_nop 4\n\ts_mov_b32 m0, %0\n\ts_nop 0\n\t"
                 "buffer_load_dwordx4 %1, %3, %4 offen lds\n\t"
                 "buffer_load_dwordx4 %2, %3, %4 offen offset:1024 lds\n\t"
                 "s_mov_b32 m0, %5\n\ts_nop 0\n\t"
                 "buffer_load_dwordx4 %1, %3, %6 offen lds\n\t"
                 "buffer_load_dwordx4 %2, %3, %6 offen offset:1024 lds"
                 ::"s"(me), "v"(ev[0]), "v"(ev[1]), "s"(re), "s"(se), "s"(me + 2048), "s"(se + 16 * SKV * 2) : "memory");
  };

  if (l < 2) {
    {
      const u32x4 rp = srd(l == 0 ? E : F, PROJ_BYTES);
      asm volatile("s_nop 4\n\ts_mov_b32 m0, %0\n\ts_nop 0\n\tbuffer_load_dword %1, %2, %3 offen lds"
                   ::"s"(lb + PF_OFF + l * 256), "v"(lane * 128), "s"(rp), "s"(rot * 8192) : "memory");
    }
    issue_x(0, 0); issue_e(0, XB);
    if (l == 1) {
      const u32x4 rq = srd(Q + (size_t)bh * (SQ * DH * 2), SQ * DH * 2);
      asm volatile("s_nop 4\n\ts_mov_b32 m0, %0\n\ts_nop 0\n\tbuffer_load_dwordx4 %1, %2, 0 offen lds" ::"s"(lb + Q_OFF), "v"(lane * 16), "s"(rq) : "memory");
    }
#pragma unroll
    for (int j = 1; j < DEPTH - 1; ++j) { issue_x(j, j * SLOT); issue_e(j, j * SLOT + XB); }
  }

  if (__builtin_amdgcn_readfirstlane(tid) < 128) {
    unsigned ps = (DEPTH - 1) * SLOT;
#pragma unroll 1
    for (int i = 0; i < 2 * NR - TW - DEPTH + 1; ++i) {
      if (i > FTR && i < FTR + DEPTH - 1) wait_vm<(DEPTH - 2) * NI + 4 * TW>();
      else wait_vm<(DEPTH - 2) * NI>();
      barrier();
      issue_x(i + DEPTH - 1, ps); issue_e(i + DEPTH - 1, ps + XB);
      if (i == FTR) {
#pragma unroll
        for (int u = 0; u < TW; ++u) issue_e(2 * NR - TW + u, FT_OFF + u * EB);
      }
      ps = ps == (DEPTH - 1) * SLOT ? 0u : ps + SLOT;
    }
    static_assert(NR == 32 && DEPTH == 4 && TW == 3, "tail schedule");
    wait_vm<2 * NI>(); barrier(); issue_x(61, 1 * SLOT);
    wait_vm<NI + 8>(); barrier(); issue_x(62, 2 * SLOT); issue_x(63, FT_OFF);
    wait_vm<24>(); barrier();
    wait_vm<16>(); barrier();
    wait_vm<8>(); barrier();
    wait_vm<0>(); barrier();
    return;
  }

  const int h = lane >> 5, m = lane & 31;
  const int blk = (lane >> 4) & 1, q4 = (lane & 15) >> 2, p4 = lane & 3;
  unsigned xr[2];
#pragma unroll
  for (int t = 0; t < 2; ++t) xr[t] = lb + 256 * (8 * h + 4 * t + q4) + 16 * (((w ^ q4) << 2) | ((2 * blk + (p4 >> 1)) ^ (2 * h + t))) + 8 * (p4 & 1);
  const unsigned er = lb + XB + m * EL + 16 * (h ^ ((m >> 1) & 7));
  const f32x16 zero16 = {0.f, 0.f, 0.f, 0.f, 0.f, 0.f, 0.f, 0.f, 0.f, 0.f, 0.f, 0.f, 0.f, 0.f, 0.f, 0.f};
  f32x16 acc[2] = {zero16, zero16};
  unsigned so = 0;
  auto round = [&](bool isK) {
    barrier();
    const unsigned xa0 = xr[0] + so, xa1 = xr[1] + so, ea = er + so;
    u32x4 xf[KS], e0[KS], e1[KS];
#pragma unroll
    for (int ks = 0; ks < KS; ++ks) {
      xf[ks] = trd2(xa0 + 4096 * ks, xa1 + 4096 * ks);
      e0[ks] = *lp<const u32x4>(ea ^ (32 * ks));
      e1[ks] = *lp<const u32x4>((ea ^ (32 * ks)) + 32 * EL);
      __builtin_amdgcn_sched_barrier(0);
    }
#pragma unroll
    for (int ks = 0; ks < KS; ++ks) {
      acc[0] = isK ? mfma(xf[ks], e0[ks], acc[0]) : mfma(e0[ks], xf[ks], acc[0]);
      acc[1] = isK ? mfma(xf[ks], e1[ks], acc[1]) : mfma(e1[ks], xf[ks], acc[1]);
    }
    __builtin_amdgcn_sched_barrier(0);
    wait_lds();
    so = so == (DEPTH - 1) * SLOT ? 0u : so + SLOT;
  };

#pragma unroll 1
  for (int i = 0; i < NR; ++i) round(true);

  {
    LAS float* sc = lp<float>(lb + SC_OFF);
    const unsigned qa = lb + Q_OFF + (m & 3) * 256 + (32 * w + 4 * h) * 2;
    const u32x4 qf0 = rd2(qa), qf1 = rd2(qa + 32);
    f32x16 s0 = mfma(qf0, cvt8(acc[0], 0), zero16), s1 = mfma(qf0, cvt8(acc[1], 0), zero16);
    s0 = mfma(qf1, cvt8(acc[0], 1), s0); s1 = mfma(qf1, cvt8(acc[1], 1), s1);
    if (h == 0) {
#pragma unroll
      for (int qi = 0; qi < SQ; ++qi) { sc[(w * SQ + qi) * RR + m] = s0[qi]; sc[(w * SQ + qi) * RR + 32 + m] = s1[qi]; }
    }
    wait_lds();
  }
  acc[0] = zero16; acc[1] = zero16;
  round(false);
  {
    const LAS float* sc = lp<const float>(lb + SC_OFF) + w * RR + lane;
    const float s = ((sc[0] + sc[SQ * RR]) + (sc[2 * SQ * RR] + sc[3 * SQ * RR])) * (0.08838834764831845f * 1.4426950408889634f);
    const float e = __builtin_amdgcn_exp2f(s - wave_max(s));
    const float p = e * __builtin_amdgcn_rcpf(wave_sum(e));
    *lp<unsigned short>(lb + P_OFF + (w * RR + lane) * 2) = (unsigned short)pk(p, p);
    wait_lds();
  }
  u32x4 wf[TW][KS];
  auto wt = [&](int u, int c) {
    f32x16 d = zero16;
    unsigned fa[2];
#pragma unroll
    for (int t = 0; t < 2; ++t)
      fa[t] = lb + FT_OFF + u * EB + EL * (8 * h + 4 * t + q4) + 16 * ((4 * c + 2 * blk + (p4 >> 1)) ^ (4 * h + 2 * t + (q4 >> 1))) + 8 * (p4 & 1);
#pragma unroll
    for (int ks = 0; ks < RR / 16; ++ks)
      d = mfma(trd2(fa[0] + 16 * EL * ks, fa[1] + 16 * EL * ks), *lp<const u32x4>(lb + P_OFF + (m & 3) * (RR * 2) + (16 * ks + 8 * h) * 2), d);
    wf[u][2 * c] = cvt8(d, 0); wf[u][2 * c + 1] = cvt8(d, 1);
    wait_lds();
  };
#pragma unroll 1
  for (int i = NR + 1; i < 2 * NR - TW; ++i) {
    round(false);
#pragma unroll
    for (int u = 0; u < TW; ++u) {
      if (i == FTR + DEPTH + 1 + 2 * u) wt(u, 0);
      if (i == FTR + DEPTH + 2 + 2 * u) wt(u, 1);
    }
  }
  f32x16 o;
  {
    const unsigned pa = lb + P_OFF + (m & 3) * (RR * 2) + 4 * h * 2;
    o = mfma(cvt8(acc[0], 0), rd2(pa), zero16);
    o = mfma(cvt8(acc[1], 0), rd2(pa + 64), o);
    o = mfma(cvt8(acc[0], 1), rd2(pa + 32), o);
    o = mfma(cvt8(acc[1], 1), rd2(pa + 96), o);
  }
  char* op = O + (size_t)bh * (SQ * DH * 2) + (m & 3) * (DH * 2) + (32 * w + 4 * (m & 4) + 8 * h) * 2;
  asm volatile("" : "+v"(op));
#pragma unroll
  for (int u = 0; u < TW; ++u) {
    unsigned ta[2];
#pragma unroll
    for (int t = 0; t < 2; ++t)
      ta[t] = lb + (u == TW - 1 ? FT_OFF : (1 + u) * SLOT) + 256 * (8 * t + 4 * h + q4) + 16 * (((w ^ q4) << 2) | ((2 * blk + (p4 >> 1)) ^ (2 * t + h))) + 8 * (p4 & 1);
    asm volatile("" : "+v"(ta[0]), "+v"(ta[1]));
    barrier();
    u32x4 xf[KS];
#pragma unroll
    for (int ks = 0; ks < KS; ++ks) { xf[ks] = trd2(ta[0] + 4096 * ks, ta[1] + 4096 * ks); __builtin_amdgcn_sched_barrier(0); }
#pragma unroll
    for (int ks = 0; ks < KS; ++ks) o = mfma(xf[ks], wf[u][ks], o);
    __builtin_amdgcn_sched_barrier(0);
  }
  {
    const bool up = (m & 4) != 0;
    const unsigned a0 = pk(up ? o[8] : o[0], up ? o[9] : o[1]), a1 = pk(up ? o[10] : o[2], up ? o[11] : o[3]);
    const unsigned b0 = pk(up ? o[12] : o[4], up ? o[13] : o[5]), b1 = pk(up ? o[14] : o[6], up ? o[15] : o[7]);
    const auto s0 = __builtin_amdgcn_permlane32_swap(a0, b0, false, false), s1 = __builtin_amdgcn_permlane32_swap(a1, b1, false, false);
    if (m < 8) *(__attribute__((address_space(1))) u32x4*)op = u32x4{s0[0], s1[0], s0[1], s1[1]};
  }
}

extern "C" void kernel_launch(void* const* d_in, const int* in_sizes, int n_in, void* d_out, int out_size, void* d_ws, size_t ws_size,
                              hipStream_t stream) {
  linformer_kernel<<<256, 384, 0, stream>>>((const char*)d_in[0], (const char*)d_in[1], (const char*)d_in[2], (const char*)d_in[3],
                                           (const char*)d_in[4], (char*)d_out);
}
```

```cpp
#include <hip/hip_runtime.h>
#include <hip/hip_bf16.h>
#include <type_traits>

typedef unsigned u32x4 __attribute__((ext_vector_type(4)));
typedef unsigned u32x2 __attribute__((ext_vector_type(2)));
typedef unsigned u32x8 __attribute__((ext_vector_type(8)));
typedef float f32x4 __attribute__((ext_vector_type(4)));
typedef float f32x16 __attribute__((ext_vector_type(16)));
typedef short s16x4 __attribute__((ext_vector_type(4)));
typedef __bf16 bf16x8 __attribute__((ext_vector_type(8)));
typedef __bf16 bf16x2 __attribute__((ext_vector_type(2)));
#define DI __device__ __forceinline__
#define LAS __attribute__((address_space(3)))

#define DEPTH 4
#define TW 3
#define FTR (NR + 4)
#define VTLB 0
#define XTRA 1
#define PF 5
#define STPOL ""
constexpr int SKV = 2048, DH = 128, RR = 64, SQ = 4, NW = 4;
constexpr int RN = 64;
constexpr int HEAD_BYTES = SKV * DH * 2;
constexpr int NR = SKV / RN;
constexpr int XB = RN * 256;
constexpr int EL = RN * 2;
constexpr int EB = RR * EL;
constexpr int SLOT = XB + EB;
constexpr int NI = 12;
constexpr int KS = RN / 16;
constexpr int FT_OFF = DEPTH * SLOT;
constexpr int Q_OFF = FT_OFF + TW * EB, SC_OFF = Q_OFF + 1024, P_OFF = SC_OFF + NW * SQ * RR * 4, ST_OFF = P_OFF + 512, PF_OFF = ST_OFF + 1024,
              LDS_USED = PF_OFF + 512, LDS_BYTES = LDS_USED > 83968 ? LDS_USED : 83968;
static_assert(LDS_BYTES <= 163840, "LDS");
static_assert((DEPTH - 1) * NI + 4 * TW + 2 <= 63 && TW >= 1 && TW <= DEPTH - 1, "vmcnt / tail");

DI f32x16 mfma(u32x4 a, u32x4 b, f32x16 c) {
  return __builtin_amdgcn_mfma_f32_32x32x16_bf16(__builtin_bit_cast(bf16x8, a), __builtin_bit_cast(bf16x8, b), c, 0, 0, 0);
}
DI unsigned pk(float lo, float hi) {
  bf16x2 v = {(__bf16)lo, (__bf16)hi};
  return __builtin_bit_cast(unsigned, v);
}
template <typename T> DI LAS T* lp(unsigned a) { return (LAS T*)(size_t)a; }
DI u32x2 trd(unsigned a) { return __builtin_bit_cast(u32x2, __builtin_amdgcn_ds_read_tr16_b64_v4i16(lp<s16x4>(a))); }
DI u32x4 trd2(unsigned a0, unsigned a1) { const u32x2 x = trd(a0), y = trd(a1); return u32x4{x[0], x[1], y[0], y[1]}; }
DI u32x4 srd(const char* p, unsigned bytes) {
  const unsigned long long a = (unsigned long long)p;
  return u32x4{(unsigned)a, (unsigned)(a >> 32) & 0xffffu, bytes, 0x00020000u};
}
template <int N> DI void wait_vm() { asm volatile("s_waitcnt vmcnt(%0)" ::"n"(N) : "memory"); }
template <int CTRL> DI float dpp(float x) { return __builtin_bit_cast(float, __builtin_amdgcn_mov_dpp(__builtin_bit_cast(int, x), CTRL, 0xf, 0xf, true)); }
DI float wave_max(float x) {
  x = fmaxf(x, dpp<0x128>(x)); x = fmaxf(x, dpp<0x124>(x)); x = fmaxf(x, dpp<0x122>(x)); x = fmaxf(x, dpp<0x121>(x));
  auto s = __builtin_amdgcn_permlane16_swap(__float_as_uint(x), __float_as_uint(x), false, false);
  x = fmaxf(__uint_as_float(s[0]), __uint_as_float(s[1]));
  auto t = __builtin_amdgcn_permlane32_swap(__float_as_uint(x), __float_as_uint(x), false, false);
  return fmaxf(__uint_as_float(t[0]), __uint_as_float(t[1]));
}
DI float wave_sum(float x) {
  x += dpp<0x128>(x); x += dpp<0x124>(x); x += dpp<0x122>(x); x += dpp<0x121>(x);
  auto s = __builtin_amdgcn_permlane16_swap(__float_as_uint(x), __float_as_uint(x), false, false);
  x = __uint_as_float(s[0]) + __uint_as_float(s[1]);
  auto t = __builtin_amdgcn_permlane32_swap(__float_as_uint(x), __float_as_uint(x), false, false);
  return __uint_as_float(t[0]) + __uint_as_float(t[1]);
}
DI u32x4 cvt8(const f32x16& x, int s) {
  return u32x4{pk(x[8 * s], x[8 * s + 1]), pk(x[8 * s + 2], x[8 * s + 3]), pk(x[8 * s + 4], x[8 * s + 5]), pk(x[8 * s + 6], x[8 * s + 7])};
}
template <int V> using IC = std::integral_constant<int, V>;

#define STAMP_ARG
#define STAMP_AT(i)
__global__ __launch_bounds__(512) void linformer_kernel(const char* __restrict__ Q_, const char* __restrict__ K_, const char* __restrict__ V_,
                                                         const char* __restrict__ E_, const char* __restrict__ F_, char* __restrict__ O_ STAMP_ARG) {
  __shared__ __attribute__((aligned(1024))) char lds[LDS_BYTES];
  u32x8 ka0; u32x4 ka1;
  const auto kap = __builtin_amdgcn_kernarg_segment_ptr();
  asm volatile("s_load_dwordx8 %0, %2, 0x0\n\ts_load_dwordx4 %1, %2, 0x20" : "=&s"(ka0), "=&s"(ka1) : "s"(kap));
  const int tid = threadIdx.x, lane = tid & 63, l = __builtin_amdgcn_readfirstlane(tid >> 6), w = l - 2;
  const int bid = blockIdx.x;
  const int bh = (bid & 7) * 4 + ((bid >> 3) & 3) + (bid >> 5) * 32;
  const unsigned lb = (unsigned)(size_t)(LAS char*)lds;
  const int rot = (bid >> 3) & 31;
  STAMP_AT(0);

  constexpr int IEND = 2 * NR - TW - DEPTH + 1;
      unsigned xv[4], ev[2];
      {
        const int g = lane >> 4, c = lane & 15;
  #pragma unroll
        for (int u = 0; u < 4; ++u) xv[u] = (unsigned)(l * 8192 + g * 256 + ((c ^ ((g << 2) | u)) << 4));
  #pragma unroll
        for (int u = 0; u < 2; ++u) {
          const int row = l * 32 + u * 8 + (lane >> 3), pos = lane & 7;
          ev[u] = (unsigned)(row * (SKV * 2) + ((pos ^ ((row >> 1) & 7)) << 4) - u * 1024);
        }
      }
      asm volatile("" ::"v"(xv[0]), "v"(xv[1]), "v"(xv[2]), "v"(xv[3]), "v"(ev[0]), "v"(ev[1]));
  asm volatile("s_waitcnt lgkmcnt(0)" : "+s"(ka0), "+s"(ka1) : "s"(kap));
  auto mkp = [](unsigned lo, unsigned hi) { return (char*)(((unsigned long long)hi << 32) | lo); };
  const char *Q = mkp(ka0[0], ka0[1]), *K = mkp(ka0[2], ka0[3]), *V = mkp(ka0[4], ka0[5]), *E = mkp(ka0[6], ka0[7]), *F = mkp(ka1[0], ka1[1]);
  char* O = mkp(ka1[2], ka1[3]);
  const char* kp = K + (size_t)bh * HEAD_BYTES;
  const char* vp = V + (size_t)bh * HEAD_BYTES;
  auto issue_x = [&](int j, int slot, unsigned dst) {
        const u32x4 rx = srd(j >= NR ? vp : kp, HEAD_BYTES);
        const unsigned sx = ((j + rot) & (NR - 1)) * XB, mx = lb + dst + l * 8192;
        asm volatile("s_nop 4\n\ts_mov_b32 m0, %0\n\ts_nop 0\n\t"
                     "buffer_load_dwordx4 %1, %5, %6 offen nt lds\n\t"
                     "buffer_load_dwordx4 %2, %5, %6 offen offset:1024 nt lds\n\t"
                     "buffer_load_dwordx4 %3, %5, %6 offen offset:2048 nt lds\n\t"
                     "buffer_load_dwordx4 %4, %5, %6 offen offset:3072 nt lds\n\t"
                     "s_mov_b32 m0, %7\n\ts_nop 0\n\t"
                     "buffer_load_dwordx4 %1, %5, %8 offen nt lds\n\t"
                     "buffer_load_dwordx4 %2, %5, %8 offen offset:1024 nt lds\n\t"
                     "buffer_load_dwordx4 %3, %5, %8 offen offset:2048 nt lds\n\t"
                     "buffer_load_dwordx4 %4, %5, %8 offen offset:3072 nt lds"
                     ::"s"(mx), "v"(xv[0]), "v"(xv[1]), "v"(xv[2]), "v"(xv[3]), "s"(rx), "s"(sx), "s"(mx + 4096), "s"(sx + 4096) : "memory");
      };
      auto issue_e = [&](int j, unsigned dst) {
        const u32x4 re = srd(j >= NR ? F : E, RR * SKV * 2);
        const unsigned se = ((j + rot) & (NR - 1)) * EL, me = lb + dst + l * 4096;
        asm volatile("s_nop 4\n\ts_mov_b32 m0, %0\n\ts_nop 0\n\t"
                     "buffer_load_dwordx4 %1, %3, %4 offen lds\n\t"
                     "buffer_load_dwordx4 %2, %3, %4 offen offset:1024 lds\n\t"
                     "s_mov_b32 m0, %5\n\ts_nop 0\n\t"
                     "buffer_load_dwordx4 %1, %3, %6 offen lds\n\t"
                     "buffer_load_dwordx4 %2, %3, %6 offen offset:1024 lds"
                     ::"s"(me), "v"(ev[0]), "v"(ev[1]), "s"(re), "s"(se), "s"(me + 2048), "s"(se + 16 * SKV * 2) : "memory");
      };
  if (l < 2) {
    if (PF == 5) {
      const u32x4 rp = srd(l == 0 ? E : F, RR * SKV * 2);
      asm volatile("s_nop 4\n\ts_mov_b32 m0, %0\n\ts_nop 0\n\tbuffer_load_dword %1, %2, %3 offen lds"
                   ::"s"(lb + PF_OFF + l * 256), "v"(lane * 128), "s"(rp), "s"(rot * 8192) : "memory");
    }
    issue_x(0, 0, 0); issue_e(0, XB);
    if (l == 1) {
      const u32x4 rq = srd(Q + (size_t)bh * (SQ * DH * 2), SQ * DH * 2);
      asm volatile("s_nop 4\n\ts_mov_b32 m0, %0\n\ts_nop 0\n\tbuffer_load_dwordx4 %1, %2, 0 offen lds" ::"s"(lb + Q_OFF), "v"(lane * 16), "s"(rq) : "memory");
    }
#pragma unroll
    for (int j = 1; j < DEPTH - 1; ++j) { issue_x(j, j, j * SLOT); issue_e(j, j * SLOT + XB); }
  }
  if (__builtin_amdgcn_readfirstlane(tid) < 128) {
    if (VTLB) {
      unsigned dummy;
      asm volatile("s_load_dword %0, %1, 0x0\n\ts_waitcnt lgkmcnt(0)" : "=&s"(dummy) : "s"(vp) : "memory");
    }
    constexpr int WF = (DEPTH - 2) * NI;
    int ps = DEPTH - 1;
#pragma unroll 1
    for (int i = 0; i < IEND; ++i) {
      if (i > FTR && i < FTR + DEPTH - 1) wait_vm<WF + 4 * TW>(); else wait_vm<WF>();
      __builtin_amdgcn_s_barrier();
      issue_x(i + DEPTH - 1, ps, ps * SLOT); issue_e(i + DEPTH - 1, ps * SLOT + XB);
      if (i == FTR) {
#pragma unroll
        for (int u = 0; u < TW; ++u) issue_e(2 * NR - TW + u, FT_OFF + u * EB);
      }
      ps = ps == DEPTH - 1 ? 0 : ps + 1;
    }
    static_assert(DEPTH == 4 && TW == 3, "tail schedule below");
    wait_vm<2 * NI>(); __builtin_amdgcn_s_barrier(); issue_x(2 * NR - 3, 1, 1 * SLOT);
    wait_vm<NI + 8>(); __builtin_amdgcn_s_barrier(); issue_x(2 * NR - 2, 2, 2 * SLOT);
    if (XTRA) issue_x(2 * NR - 1, 3, FT_OFF);
    if (XTRA) wait_vm<24>(); else wait_vm<16>();
    __builtin_amdgcn_s_barrier();
    if (!XTRA) issue_x(2 * NR - 1, 3, 3 * SLOT);
    wait_vm<16>(); __builtin_amdgcn_s_barrier();
    wait_vm<8>(); __builtin_amdgcn_s_barrier();
    wait_vm<0>(); __builtin_amdgcn_s_barrier();
    return;
  }

  const int h = lane >> 5, m = lane & 31;
  const int blk = (lane >> 4) & 1, q4 = (lane & 15) >> 2, p4 = lane & 3;
  unsigned xr[2], er;
#pragma unroll
  for (int t = 0; t < 2; ++t) xr[t] = lb + 256 * (8 * h + 4 * t + q4) + 16 * (((w ^ q4) << 2) | ((2 * blk + (p4 >> 1)) ^ (2 * h + t))) + 8 * (p4 & 1);
  er = lb + XB + m * EL + 16 * (h ^ ((m >> 1) & 7));
  f32x16 acc[2];
  int slot = 0;
  auto round = [&](int i, bool isK) {
    __builtin_amdgcn_s_barrier();
    asm volatile("" ::: "memory");
    STAMP_AT(1 + i);
    const unsigned so = slot * SLOT;
    const unsigned xa0 = xr[0] + so, xa1 = xr[1] + so, ea = er + so;
    u32x4 xf[KS], e0[KS], e1[KS];
#pragma unroll
    for (int ks = 0; ks < KS; ++ks) {
      xf[ks] = trd2(xa0 + 4096 * ks, xa1 + 4096 * ks);
      e0[ks] = *lp<const u32x4>(ea ^ (32 * ks));
      e1[ks] = *lp<const u32x4>((ea ^ (32 * ks)) + 32 * EL);
      __builtin_amdgcn_sched_barrier(0);
    }
#pragma unroll
    for (int ks = 0; ks < KS; ++ks) {
      acc[0] = isK ? mfma(xf[ks], e0[ks], acc[0]) : mfma(e0[ks], xf[ks], acc[0]);
      acc[1] = isK ? mfma(xf[ks], e1[ks], acc[1]) : mfma(e1[ks], xf[ks], acc[1]);
    }
    __builtin_amdgcn_sched_barrier(0);
    asm volatile("s_waitcnt lgkmcnt(0)" ::: "memory");
    slot = slot == DEPTH - 1 ? 0 : slot + 1;
  };
  const f32x16 zero16 = {0.f, 0.f, 0.f, 0.f, 0.f, 0.f, 0.f, 0.f, 0.f, 0.f, 0.f, 0.f, 0.f, 0.f, 0.f, 0.f};

  acc[0] = zero16; acc[1] = zero16;
#pragma unroll 1
  for (int i = 0; i < NR; ++i) round(i, true);

  {
    LAS float* sc = lp<float>(lb + SC_OFF);
    u32x4 qf[2];
#pragma unroll
    for (int s = 0; s < 2; ++s) {
      const unsigned qa = lb + Q_OFF + (m & 3) * 256 + (32 * w + 16 * s + 4 * h) * 2;
      const u32x2 a = *lp<const u32x2>(qa), b = *lp<const u32x2>(qa + 16);
      qf[s] = u32x4{a[0], a[1], b[0], b[1]};
    }
    f32x16 s0 = mfma(qf[0], cvt8(acc[0], 0), zero16), s1 = mfma(qf[0], cvt8(acc[1], 0), zero16);
    s0 = mfma(qf[1], cvt8(acc[0], 1), s0); s1 = mfma(qf[1], cvt8(acc[1], 1), s1);
    if (h == 0) {
#pragma unroll
      for (int qi = 0; qi < 4; ++qi) { sc[(w * SQ + qi) * RR + m] = s0[qi]; sc[(w * SQ + qi) * RR + 32 + m] = s1[qi]; }
    }
    asm volatile("s_waitcnt lgkmcnt(0)" ::: "memory");
  }
  acc[0] = zero16; acc[1] = zero16;
  round(NR, false);
  {
    const LAS float* sc = lp<const float>(lb + SC_OFF) + w * RR + lane;
    const float s = ((sc[0] + sc[SQ * RR]) + (sc[2 * SQ * RR] + sc[3 * SQ * RR])) * (0.08838834764831845f * 1.4426950408889634f);
    const float e = __builtin_amdgcn_exp2f(s - wave_max(s));
    const float p = e * __builtin_amdgcn_rcpf(wave_sum(e));
    *lp<unsigned short>(lb + P_OFF + (w * RR + lane) * 2) = (unsigned short)pk(p, p);
    asm volatile("s_waitcnt lgkmcnt(0)" ::: "memory");
  }
  u32x4 wf[TW][KS];
  auto wt = [&](int u, int c) {
    f32x16 d = zero16;
    unsigned fa[2];
#pragma unroll
    for (int t = 0; t < 2; ++t)
      fa[t] = lb + FT_OFF + u * EB + EL * (8 * h + 4 * t + q4) + 16 * ((4 * c + 2 * blk + (p4 >> 1)) ^ (4 * h + 2 * t + (q4 >> 1))) + 8 * (p4 & 1);
#pragma unroll
    for (int ks = 0; ks < 4; ++ks)
      d = mfma(trd2(fa[0] + 16 * EL * ks, fa[1] + 16 * EL * ks), *lp<const u32x4>(lb + P_OFF + (m & 3) * (RR * 2) + (16 * ks + 8 * h) * 2), d);
    wf[u][2 * c] = cvt8(d, 0); wf[u][2 * c + 1] = cvt8(d, 1);
    asm volatile("s_waitcnt lgkmcnt(0)" ::: "memory");
  };
#pragma unroll 1
  for (int i = NR + 1; i < 2 * NR - TW; ++i) {
    round(i, false);
#pragma unroll
    for (int u = 0; u < TW; ++u) {
      if (i == FTR + DEPTH + 1 + 2 * u) wt(u, 0);
      if (i == FTR + DEPTH + 2 + 2 * u) wt(u, 1);
    }
  }
  u32x4 pf[4];
#pragma unroll
  for (int s = 0; s < 4; ++s) {
    const unsigned pa = lb + P_OFF + (m & 3) * (RR * 2) + (16 * s + 4 * h) * 2;
    const u32x2 a = *lp<const u32x2>(pa), b = *lp<const u32x2>(pa + 16);
    pf[s] = u32x4{a[0], a[1], b[0], b[1]};
  }
  f32x16 o = mfma(cvt8(acc[0], 0), pf[0], zero16);
  o = mfma(cvt8(acc[1], 0), pf[2], o);
  o = mfma(cvt8(acc[0], 1), pf[1], o);
  o = mfma(cvt8(acc[1], 1), pf[3], o);
  unsigned xt[2];
#pragma unroll
  for (int t = 0; t < 2; ++t) xt[t] = lb + 256 * (8 * t + 4 * h + q4) + 16 * (((w ^ q4) << 2) | ((2 * blk + (p4 >> 1)) ^ (2 * t + h))) + 8 * (p4 & 1);
  char* op = O + (size_t)bh * (SQ * DH * 2) + (m & 3) * (DH * 2) + (32 * w + 4 * (m & 4) + 8 * h) * 2;
  asm volatile("" : "+v"(op));
  auto tail = [&](auto uc) {
    constexpr int u = decltype(uc)::value, i = 2 * NR - TW + u;
    constexpr unsigned so = (XTRA && u == 2) ? FT_OFF : (i % DEPTH) * SLOT;
    unsigned ta0 = xt[0] + so, ta1 = xt[1] + so;
    asm volatile("" : "+v"(ta0), "+v"(ta1));
    __builtin_amdgcn_s_barrier();
    asm volatile("" ::: "memory");
    STAMP_AT(1 + i);
    u32x4 xf[KS];
#pragma unroll
    for (int ks = 0; ks < KS; ++ks) { xf[ks] = trd2(ta0 + 4096 * ks, ta1 + 4096 * ks); __builtin_amdgcn_sched_barrier(0); }
#pragma unroll
    for (int ks = 0; ks < KS; ++ks) o = mfma(xf[ks], wf[u][ks], o);
    __builtin_amdgcn_sched_barrier(0);
  };
  tail(IC<0>{});
  if constexpr (TW >= 2) tail(IC<1>{});
  if constexpr (TW >= 3) tail(IC<2>{});
  if constexpr (TW >= 4) tail(IC<3>{});
  {
    const bool up = (m & 4) != 0;
    const unsigned a0 = pk(up ? o[8] : o[0], up ? o[9] : o[1]), a1 = pk(up ? o[10] : o[2], up ? o[11] : o[3]);
    const unsigned b0 = pk(up ? o[12] : o[4], up ? o[13] : o[5]), b1 = pk(up ? o[14] : o[6], up ? o[15] : o[7]);
    const auto s0 = __builtin_amdgcn_permlane32_swap(a0, b0, false, false), s1 = __builtin_amdgcn_permlane32_swap(a1, b1, false, false);
    const u32x4 val = {s0[0], s1[0], s0[1], s1[1]};
    if (m < 8) asm volatile("global_store_dwordx4 %0, %1, off " STPOL "\n\ts_nop 1" ::"v"(op), "v"(val) : "memory");
  }
}

extern "C" void kernel_launch(void* const* d_in, const int* in_sizes, int n_in, void* d_out, int out_size, void* d_ws, size_t ws_size,
                              hipStream_t stream) {
  linformer_kernel<<<256, 384, 0, stream>>>((const char*)d_in[0], (const char*)d_in[1], (const char*)d_in[2], (const char*)d_in[3],
                                           (const char*)d_in[4], (char*)d_out
  );
}
```

```cpp
#include <hip/hip_runtime.h>
#include <hip/hip_bf16.h>

typedef unsigned u32x2 __attribute__((ext_vector_type(2)));
typedef unsigned u32x4 __attribute__((ext_vector_type(4)));
typedef unsigned u32x8 __attribute__((ext_vector_type(8)));
typedef float f32x16 __attribute__((ext_vector_type(16)));
typedef short s16x4 __attribute__((ext_vector_type(4)));
typedef __bf16 bf16x8 __attribute__((ext_vector_type(8)));
typedef __bf16 bf16x2 __attribute__((ext_vector_type(2)));
#define DI __device__ __forceinline__
#define LAS __attribute__((address_space(3)))

constexpr int SKV = 2048, DH = 128, RR = 64, SQ = 4, NW = 4;
constexpr int HEAD_BYTES = SKV * DH * 2, PROJ_BYTES = RR * SKV * 2;
constexpr int RN = 64;
constexpr int NR = SKV / RN;
constexpr int XB = RN * 256;
constexpr int EL = RN * 2;
constexpr int EB = RR * EL;
constexpr int SLOT = XB + EB, DEPTH = 4;
constexpr int NI = 12;
constexpr int KS = RN / 16;
constexpr int TW = 3;
constexpr int FTR = NR + 4;
constexpr int FT_OFF = DEPTH * SLOT;
constexpr int Q_OFF = FT_OFF + TW * EB, SC_OFF = Q_OFF + 1024, P_OFF = SC_OFF + NW * SQ * RR * 4, PF_OFF = P_OFF + 512, LDS_BYTES = PF_OFF + 512;
static_assert(LDS_BYTES <= 163840 && LDS_BYTES > 81920, "one workgroup per CU");
static_assert((DEPTH - 1) * NI + 4 * TW + 2 <= 63 && TW * EB >= XB, "vmcnt range / reuse of the F area");

DI f32x16 mfma(u32x4 a, u32x4 b, f32x16 c) {
  return __builtin_amdgcn_mfma_f32_32x32x16_bf16(__builtin_bit_cast(bf16x8, a), __builtin_bit_cast(bf16x8, b), c, 0, 0, 0);
}
DI unsigned pk(float lo, float hi) {
  bf16x2 v = {(__bf16)lo, (__bf16)hi};
  return __builtin_bit_cast(unsigned, v);
}
DI u32x4 cvt8(const f32x16& x, int s) {
  return u32x4{pk(x[8 * s], x[8 * s + 1]), pk(x[8 * s + 2], x[8 * s + 3]), pk(x[8 * s + 4], x[8 * s + 5]), pk(x[8 * s + 6], x[8 * s + 7])};
}
template <typename T> DI LAS T* lp(unsigned a) { return (LAS T*)(size_t)a; }
DI u32x4 trd2(unsigned a0, unsigned a1) {
  const u32x2 x = __builtin_bit_cast(u32x2, __builtin_amdgcn_ds_read_tr16_b64_v4i16(lp<s16x4>(a0)));
  const u32x2 y = __builtin_bit_cast(u32x2, __builtin_amdgcn_ds_read_tr16_b64_v4i16(lp<s16x4>(a1)));
  return u32x4{x[0], x[1], y[0], y[1]};
}
DI u32x4 rd2(unsigned a) {
  const u32x2 x = *lp<const u32x2>(a), y = *lp<const u32x2>(a + 16);
  return u32x4{x[0], x[1], y[0], y[1]};
}
DI u32x4 srd(const char* p, unsigned bytes) {
  const unsigned long long a = (unsigned long long)p;
  return u32x4{(unsigned)a, (unsigned)(a >> 32) & 0xffffu, bytes, 0x00020000u};
}
template <int N> DI void wait_vm() { asm volatile("s_waitcnt vmcnt(%0)" ::"n"(N) : "memory"); }
DI void wait_lds() { asm volatile("s_waitcnt lgkmcnt(0)" ::: "memory"); }
DI void barrier() { __builtin_amdgcn_s_barrier(); asm volatile("" ::: "memory"); }
template <int CTRL> DI float dpp(float x) { return __builtin_bit_cast(float, __builtin_amdgcn_mov_dpp(__builtin_bit_cast(int, x), CTRL, 0xf, 0xf, true)); }
DI float wave_max(float x) {
  x = fmaxf(x, dpp<0x128>(x)); x = fmaxf(x, dpp<0x124>(x)); x = fmaxf(x, dpp<0x122>(x)); x = fmaxf(x, dpp<0x121>(x));
  const auto s = __builtin_amdgcn_permlane16_swap(__float_as_uint(x), __float_as_uint(x), false, false);
  x = fmaxf(__uint_as_float(s[0]), __uint_as_float(s[1]));
  const auto t = __builtin_amdgcn_permlane32_swap(__float_as_uint(x), __float_as_uint(x), false, false);
  return fmaxf(__uint_as_float(t[0]), __uint_as_float(t[1]));
}
DI float wave_sum(float x) {
  x += dpp<0x128>(x); x += dpp<0x124>(x); x += dpp<0x122>(x); x += dpp<0x121>(x);
  const auto s = __builtin_amdgcn_permlane16_swap(__float_as_uint(x), __float_as_uint(x), false, false);
  x = __uint_as_float(s[0]) + __uint_as_float(s[1]);
  const auto t = __builtin_amdgcn_permlane32_swap(__float_as_uint(x), __float_as_uint(x), false, false);
  return __uint_as_float(t[0]) + __uint_as_float(t[1]);
}

__global__ __launch_bounds__(512) void linformer_kernel(const char* Q_, const char* K_, const char* V_, const char* E_, const char* F_, char* O_) {
  __shared__ __attribute__((aligned(1024))) char lds[LDS_BYTES];
  u32x8 ka0; u32x4 ka1;
  const auto kap = __builtin_amdgcn_kernarg_segment_ptr();
  asm volatile("s_load_dwordx8 %0, %2, 0x0\n\ts_load_dwordx4 %1, %2, 0x20" : "=&s"(ka0), "=&s"(ka1) : "s"(kap));
  __builtin_amdgcn_sched_barrier(0);
  const int tid = threadIdx.x, lane = tid & 63, l = __builtin_amdgcn_readfirstlane(tid >> 6), w = l - 2;
  const int bid = blockIdx.x;
  const int bh = (bid & 7) * 4 + ((bid >> 3) & 3) + (bid >> 5) * 32;
  const int rot = (bid >> 3) & 31;
  const unsigned lb = (unsigned)(size_t)(LAS char*)lds;

  unsigned xv[4], ev[2];
#pragma unroll
  for (int u = 0; u < 4; ++u) xv[u] = (unsigned)(l * 8192 + (lane >> 4) * 256 + (((lane & 15) ^ (((lane >> 4) << 2) | u)) << 4));
#pragma unroll
  for (int u = 0; u < 2; ++u) {
    const int row = l * 32 + u * 8 + (lane >> 3);
    ev[u] = (unsigned)(row * (SKV * 2) + (((lane & 7) ^ ((row >> 1) & 7)) << 4) - u * 1024);
  }
  asm volatile("" ::"v"(xv[0]), "v"(xv[1]), "v"(xv[2]), "v"(xv[3]), "v"(ev[0]), "v"(ev[1]));
  asm volatile("s_waitcnt lgkmcnt(0)" : "+s"(ka0), "+s"(ka1) : "s"(kap));
  auto mkp = [](unsigned lo, unsigned hi) { return (char*)(((unsigned long long)hi << 32) | lo); };
  const char *Q = mkp(ka0[0], ka0[1]), *K = mkp(ka0[2], ka0[3]), *V = mkp(ka0[4], ka0[5]), *E = mkp(ka0[6], ka0[7]), *F = mkp(ka1[0], ka1[1]);
  char* O = mkp(ka1[2], ka1[3]);
  const char* kp = K + (size_t)bh * HEAD_BYTES;
  const char* vp = V + (size_t)bh * HEAD_BYTES;

  auto issue_x = [&](int j, unsigned dst) {
    const u32x4 rx = srd(j >= NR ? vp : kp, HEAD_BYTES);
    const unsigned sx = ((j + rot) & (NR - 1)) * XB, mx = lb + dst + l * 8192;
    asm volatile("s_nop 4\n\ts_mov_b32 m0, %0\n\ts_nop 0\n\t"
                 "buffer_load_dwordx4 %1, %5, %6 offen nt lds\n\t"
                 "buffer_load_dwordx4 %2, %5, %6 offen offset:1024 nt lds\n\t"
                 "buffer_load_dwordx4 %3, %5, %6 offen offset:2048 nt lds\n\t"
                 "buffer_load_dwordx4 %4, %5, %6 offen offset:3072 nt lds\n\t"
                 "s_mov_b32 m0, %7\n\ts_nop 0\n\t"
                 "buffer_load_dwordx4 %1, %5, %8 offen nt lds\n\t"
                 "buffer_load_dwordx4 %2, %5, %8 offen offset:1024 nt lds\n\t"
                 "buffer_load_dwordx4 %3, %5, %8 offen offset:2048 nt lds\n\t"
                 "buffer_load_dwordx4 %4, %5, %8 offen offset:3072 nt lds"
                 ::"s"(mx), "v"(xv[0]), "v"(xv[1]), "v"(xv[2]), "v"(xv[3]), "s"(rx), "s"(sx), "s"(mx + 4096), "s"(sx + 4096) : "memory");
  };
  auto issue_e = [&](int j, unsigned dst) {
    const u32x4 re = srd(j >= NR ? F : E, PROJ_BYTES);
    const unsigned se = ((j + rot) & (NR - 1)) * EL, me = lb + dst + l * 4096;
    asm volatile("s_nop 4\n\ts_mov_b32 m0, %0\n\ts_nop 0\n\t"
                 "buffer_load_dwordx4 %1, %3, %4 offen lds\n\t"
                 "buffer_load_dwordx4 %2, %3, %4 offen offset:1024 lds\n\t"
                 "s_mov_b32 m0, %5\n\ts_nop 0\n\t"
                 "buffer_load_dwordx4 %1, %3, %6 offen lds\n\t"
                 "buffer_load_dwordx4 %2, %3, %6 offen offset:1024 lds"
                 ::"s"(me), "v"(ev[0]), "v"(ev[1]), "s"(re), "s"(se), "s"(me + 2048), "s"(se + 16 * SKV * 2) : "memory");
  };

  if (l >= 2) {
    const int h = lane >> 5, m = lane & 31;
    const int blk = (lane >> 4) & 1, q4 = (lane & 15) >> 2, p4 = lane & 3;
    unsigned xr[2];
#pragma unroll
    for (int t = 0; t < 2; ++t) xr[t] = lb + 256 * (8 * h + 4 * t + q4) + 16 * (((w ^ q4) << 2) | ((2 * blk + (p4 >> 1)) ^ (2 * h + t))) + 8 * (p4 & 1);
    const unsigned er = lb + XB + m * EL + 16 * (h ^ ((m >> 1) & 7));
    const f32x16 zero16 = {0.f, 0.f, 0.f, 0.f, 0.f, 0.f, 0.f, 0.f, 0.f, 0.f, 0.f, 0.f, 0.f, 0.f, 0.f, 0.f};
    f32x16 acc[2] = {zero16, zero16};
    unsigned so = 0;
    auto round = [&](bool isK) {
      barrier();
      const unsigned xa0 = xr[0] + so, xa1 = xr[1] + so, ea = er + so;
      u32x4 xf[KS], e0[KS], e1[KS];
#pragma unroll
      for (int ks = 0; ks < KS; ++ks) {
        xf[ks] = trd2(xa0 + 4096 * ks, xa1 + 4096 * ks);
        e0[ks] = *lp<const u32x4>(ea ^ (32 * ks));
        e1[ks] = *lp<const u32x4>((ea ^ (32 * ks)) + 32 * EL);
        __builtin_amdgcn_sched_barrier(0);
      }
#pragma unroll
      for (int ks = 0; ks < KS; ++ks) {
        acc[0] = isK ? mfma(xf[ks], e0[ks], acc[0]) : mfma(e0[ks], xf[ks], acc[0]);
        acc[1] = isK ? mfma(xf[ks], e1[ks], acc[1]) : mfma(e1[ks], xf[ks], acc[1]);
      }
      __builtin_amdgcn_sched_barrier(0);
      wait_lds();
      so = so == (DEPTH - 1) * SLOT ? 0u : so + SLOT;
    };

#pragma unroll 1
    for (int i = 0; i < NR; ++i) round(true);

    {
      LAS float* sc = lp<float>(lb + SC_OFF);
      const unsigned qa = lb + Q_OFF + (m & 3) * 256 + (32 * w + 4 * h) * 2;
      const u32x4 qf0 = rd2(qa), qf1 = rd2(qa + 32);
      f32x16 s0 = mfma(qf0, cvt8(acc[0], 0), zero16), s1 = mfma(qf0, cvt8(acc[1], 0), zero16);
      s0 = mfma(qf1, cvt8(acc[0], 1), s0); s1 = mfma(qf1, cvt8(acc[1], 1), s1);
      if (h == 0) {
#pragma unroll
        for (int qi = 0; qi < SQ; ++qi) { sc[(w * SQ + qi) * RR + m] = s0[qi]; sc[(w * SQ + qi) * RR + 32 + m] = s1[qi]; }
      }
      wait_lds();
    }
    acc[0] = zero16; acc[1] = zero16;
    round(false);
    {
      const LAS float* sc = lp<const float>(lb + SC_OFF) + w * RR + lane;
      const float s = ((sc[0] + sc[SQ * RR]) + (sc[2 * SQ * RR] + sc[3 * SQ * RR])) * (0.08838834764831845f * 1.4426950408889634f);
      const float e = __builtin_amdgcn_exp2f(s - wave_max(s));
      const float p = e * __builtin_amdgcn_rcpf(wave_sum(e));
      *lp<unsigned short>(lb + P_OFF + (w * RR + lane) * 2) = (unsigned short)pk(p, p);
      wait_lds();
    }
    u32x4 wf[TW][KS];
    auto wt = [&](int u, int c) {
      f32x16 d = zero16;
      unsigned fa[2];
#pragma unroll
      for (int t = 0; t < 2; ++t)
        fa[t] = lb + FT_OFF + u * EB + EL * (8 * h + 4 * t + q4) + 16 * ((4 * c + 2 * blk + (p4 >> 1)) ^ (4 * h + 2 * t + (q4 >> 1))) + 8 * (p4 & 1);
#pragma unroll
      for (int ks = 0; ks < RR / 16; ++ks)
        d = mfma(trd2(fa[0] + 16 * EL * ks, fa[1] + 16 * EL * ks), *lp<const u32x4>(lb + P_OFF + (m & 3) * (RR * 2) + (16 * ks + 8 * h) * 2), d);
      wf[u][2 * c] = cvt8(d, 0); wf[u][2 * c + 1] = cvt8(d, 1);
      wait_lds();
    };
#pragma unroll 1
    for (int i = NR + 1; i < 2 * NR - TW; ++i) {
      round(false);
#pragma unroll
      for (int u = 0; u < TW; ++u) {
        if (i == FTR + DEPTH + 1 + 2 * u) wt(u, 0);
        if (i == FTR + DEPTH + 2 + 2 * u) wt(u, 1);
      }
    }
    f32x16 o;
    {
      const unsigned pa = lb + P_OFF + (m & 3) * (RR * 2) + 4 * h * 2;
      o = mfma(cvt8(acc[0], 0), rd2(pa), zero16);
      o = mfma(cvt8(acc[1], 0), rd2(pa + 64), o);
      o = mfma(cvt8(acc[0], 1), rd2(pa + 32), o);
      o = mfma(cvt8(acc[1], 1), rd2(pa + 96), o);
    }
    char* op = O + (size_t)bh * (SQ * DH * 2) + (m & 3) * (DH * 2) + (32 * w + 4 * (m & 4) + 8 * h) * 2;
    asm volatile("" : "+v"(op));
#pragma unroll
    for (int u = 0; u < TW; ++u) {
      unsigned ta[2];
#pragma unroll
      for (int t = 0; t < 2; ++t)
        ta[t] = lb + (u == TW - 1 ? FT_OFF : (1 + u) * SLOT) + 256 * (8 * t + 4 * h + q4) + 16 * (((w ^ q4) << 2) | ((2 * blk + (p4 >> 1)) ^ (2 * t + h))) + 8 * (p4 & 1);
      asm volatile("" : "+v"(ta[0]), "+v"(ta[1]));
      barrier();
      u32x4 xf[KS];
#pragma unroll
      for (int ks = 0; ks < KS; ++ks) { xf[ks] = trd2(ta[0] + 4096 * ks, ta[1] + 4096 * ks); __builtin_amdgcn_sched_barrier(0); }
#pragma unroll
      for (int ks = 0; ks < KS; ++ks) o = mfma(xf[ks], wf[u][ks], o);
      __builtin_amdgcn_sched_barrier(0);
    }
    {
      const bool up = (m & 4) != 0;
      const unsigned a0 = pk(up ? o[8] : o[0], up ? o[9] : o[1]), a1 = pk(up ? o[10] : o[2], up ? o[11] : o[3]);
      const unsigned b0 = pk(up ? o[12] : o[4], up ? o[13] : o[5]), b1 = pk(up ? o[14] : o[6], up ? o[15] : o[7]);
      const auto s0 = __builtin_amdgcn_permlane32_swap(a0, b0, false, false), s1 = __builtin_amdgcn_permlane32_swap(a1, b1, false, false);
      if (m < 8) *(__attribute__((address_space(1))) u32x4*)op = u32x4{s0[0], s1[0], s0[1], s1[1]};
    }
  } else {
    {
      const u32x4 rp = srd(l == 0 ? E : F, PROJ_BYTES);
      asm volatile("s_nop 4\n\ts_mov_b32 m0, %0\n\ts_nop 0\n\tbuffer_load_dword %1, %2, %3 offen lds"
                   ::"s"(lb + PF_OFF + l * 256), "v"(lane * 128), "s"(rp), "s"(rot * 8192) : "memory");
    }
    issue_x(0, 0); issue_e(0, XB);
    if (l == 1) {
      const u32x4 rq = srd(Q + (size_t)bh * (SQ * DH * 2), SQ * DH * 2);
      asm volatile("s_nop 4\n\ts_mov_b32 m0, %0\n\ts_nop 0\n\tbuffer_load_dwordx4 %1, %2, 0 offen lds" ::"s"(lb + Q_OFF), "v"(lane * 16), "s"(rq) : "memory");
    }
#pragma unroll
    for (int j = 1; j < DEPTH - 1; ++j) { issue_x(j, j * SLOT); issue_e(j, j * SLOT + XB); }
    unsigned ps = (DEPTH - 1) * SLOT;
#pragma unroll 1
    for (int i = 0; i < 2 * NR - TW - DEPTH + 1; ++i) {
      if (i > FTR && i < FTR + DEPTH - 1) wait_vm<(DEPTH - 2) * NI + 4 * TW>();
      else wait_vm<(DEPTH - 2) * NI>();
      barrier();
      issue_x(i + DEPTH - 1, ps); issue_e(i + DEPTH - 1, ps + XB);
      if (i == FTR) {
#pragma unroll
        for (int u = 0; u < TW; ++u) issue_e(2 * NR - TW + u, FT_OFF + u * EB);
      }
      ps = ps == (DEPTH - 1) * SLOT ? 0u : ps + SLOT;
    }
    static_assert(NR == 32 && DEPTH == 4 && TW == 3, "tail schedule");
    wait_vm<2 * NI>(); barrier(); issue_x(61, 1 * SLOT);
    wait_vm<NI + 8>(); barrier(); issue_x(62, 2 * SLOT); issue_x(63, FT_OFF);
    wait_vm<24>(); barrier();
    wait_vm<16>(); barrier();
    wait_vm<8>(); barrier();
    wait_vm<0>(); barrier();
  }
}

extern "C" void kernel_launch(void* const* d_in, const int* in_sizes, int n_in, void* d_out, int out_size, void* d_ws, size_t ws_size,
                              hipStream_t stream) {
  linformer_kernel<<<256, 384, 0, stream>>>((const char*)d_in[0], (const char*)d_in[1], (const char*)d_in[2], (const char*)d_in[3],
                                           (const char*)d_in[4], (char*)d_out);
}
```
